```python
import torch

def _softmax(x, axis=-1):
    return torch.softmax(x, dim=axis)
def _gelu(x):
    return torch.nn.functional.gelu(x, approximate="tanh")
def _silu(x):
    return torch.nn.functional.silu(x)
def _sigmoid(x):
    return torch.sigmoid(x)
def _layernorm(x, axis=-1, eps=1e-5):
    m = x.mean(dim=axis, keepdim=True)
    v = x.var(dim=axis, keepdim=True, unbiased=False)
    return (x - m) / torch.sqrt(v + eps)
def _scatter_add(tgt, idx, src, axis=0):
    return tgt.clone().index_add_(axis, idx, src)
def _segment_sum(src, seg, n):
    out = torch.zeros((n,) + src.shape[1:], dtype=src.dtype)
    return out.index_add_(0, seg, src)
def _pad(x, pw):
    flat = tuple(v for pair in reversed(pw) for v in pair)
    return torch.nn.functional.pad(x, flat)

kernel_name = 'parallel_sum__sliding_block_cached__mixer_channel_mlp__decode'

def setup_inputs():

    _w = (lambda a: (lambda t: t.to(torch.bfloat16) if t.is_floating_point() else t)(torch.as_tensor(a)))
    def _A_setup():
        B, Sq, D = 1, 1, 576
        Hq, Hkv, Sk, Dh = 32, 16, 16384, 128
        assert Hq % Hkv == 0, (Hq, Hkv)
        return {
            "x": _w((lambda *s: torch.randn(*s, dtype=torch.bfloat16) * 0.05)(B, Sq, D)),
            "k_cache": _w((lambda *s: torch.randn(*s, dtype=torch.bfloat16) * 3.2)(B, Hkv, Sk, Dh)),
            "v_cache": _w((lambda *s: torch.randn(*s, dtype=torch.bfloat16) * 0.4)(B, Hkv, Sk, Dh)),
            "wq": _w((lambda *s: torch.randn(*s, dtype=torch.bfloat16) * 3.2)(D, Hq * Dh) * (D ** -0.5)),
            "wo": _w((lambda *s: torch.randn(*s, dtype=torch.bfloat16) * 0.4)(Hq * Dh, D) * ((Hq * Dh) ** -0.5)),
        }
    _A_in = _A_setup()

    def _B_setup():
        B, S, D, F = 1, 1, 576, 4608
        return {
            "x": _w((lambda *s: torch.randn(*s, dtype=torch.bfloat16) * 0.05)(B, S, D)),
            "w1": _w((lambda *s: torch.randn(*s, dtype=torch.bfloat16) * 3.2)(D, F) * (D ** -0.5)),
            "b1": _w((lambda *s: torch.randn(*s, dtype=torch.bfloat16) * 1.6)(F)),
            "w2": _w((lambda *s: torch.randn(*s, dtype=torch.bfloat16) * 0.1)(F, D) * (F ** -0.5)),
            "b2": _w((lambda *s: torch.randn(*s, dtype=torch.bfloat16) * 0.1)(D)),
        }
    _B_in = _B_setup()

    return {
        "A_x": _A_in["x"],
        "A_k_cache": _A_in["k_cache"],
        "A_v_cache": _A_in["v_cache"],
        "A_wq": _A_in["wq"],
        "A_wo": _A_in["wo"],
        "B_x": _B_in["x"],
        "B_w1": _B_in["w1"],
        "B_b1": _B_in["b1"],
        "B_w2": _B_in["w2"],
        "B_b2": _B_in["b2"],
    }

def reference(A_x, A_k_cache, A_v_cache, A_wq, A_wo, B_x, B_w1, B_b1, B_w2, B_b2):
    def _A_ref(x, k_cache, v_cache, wq, wo):
        B, Sq, D = x.shape
        Hq, Dh = 32, 128
        Hkv, Sk = k_cache.shape[1], k_cache.shape[2]
        W = min(4096, max(256, Sk // 4))
        rep = Hq // Hkv
        q = (lambda x, *d: x.permute(*d))((x @ wq).reshape(B, Sq, Hq, Dh), 0, 2, 1, 3)
        k = (lambda x, n, axis: torch.repeat_interleave(x, n, dim=axis))(k_cache[:, :, Sk - W:, :], rep, axis=1)
        v = (lambda x, n, axis: torch.repeat_interleave(x, n, dim=axis))(v_cache[:, :, Sk - W:, :], rep, axis=1)
        scores = torch.einsum("bhqd,bhkd->bhqk", q, k) * (Dh ** -0.5)
        p = _softmax(scores, axis=-1)
        o = torch.einsum("bhqk,bhkd->bhqd", p, v)
        return (lambda x, *d: x.permute(*d))(o, 0, 2, 1, 3).reshape(B, Sq, Hq * Dh) @ wo

    def _B_ref(x, w1, b1, w2, b2):
        y = _layernorm(x, axis=-1)
        h = _gelu(torch.einsum("bsd,df->bsf", y, w1) + b1)
        return torch.einsum("bsf,fd->bsd", h, w2) + b2

    x = A_x
    xn = _layernorm(x, axis=-1)
    return x + _A_ref(xn, A_k_cache, A_v_cache, A_wq, A_wo) + _B_ref(xn, B_w1, B_b1, B_w2, B_b2)
```

```cpp
#include <hip/hip_runtime.h>
#include <hip/hip_bf16.h>
#include <stdint.h>

typedef unsigned short u16;
constexpr int D = 576, HQ = 32, HKV = 16, SK = 16384, DH = 128, W = 4096, F = 4608;
constexpr int NSPLIT = 16, PS = W / NSPLIT;

__device__ __forceinline__ float bf2f(u16 b) { return __builtin_bit_cast(float, (unsigned)b << 16); }
__device__ __forceinline__ float rbf(float f) {
  unsigned u = __builtin_bit_cast(unsigned, f);
  u += 0x7FFFu + ((u >> 16) & 1u);
  return __builtin_bit_cast(float, u & 0xFFFF0000u);
}
__device__ __forceinline__ u16 f2bf(float f) { return (u16)(__builtin_bit_cast(unsigned, rbf(f)) >> 16); }

__device__ float block_sum(float v, float* red) {
  for (int o = 32; o >= 1; o >>= 1) v += __shfl_xor(v, o);
  __syncthreads();
  if ((threadIdx.x & 63) == 0) red[threadIdx.x >> 6] = v;
  __syncthreads();
  float s = 0.f;
  for (int i = 0; i < (int)(blockDim.x >> 6); ++i) s += red[i];
  return s;
}

__device__ void layernorm(const float* in, float* out, float* red) {
  float s = 0.f;
  for (int i = threadIdx.x; i < D; i += blockDim.x) s += in[i];
  float mean = block_sum(s, red) * (1.0f / D);
  float s2 = 0.f;
  for (int i = threadIdx.x; i < D; i += blockDim.x) { float d = in[i] - mean; s2 += d * d; }
  float var = block_sum(s2, red) * (1.0f / D);
  float mb = rbf(mean), vb = rbf(var);
  float sd = rbf(sqrtf(rbf(vb + 1e-5f)));
  for (int i = threadIdx.x; i < D; i += blockDim.x) out[i] = rbf(rbf(in[i] - mb) / sd);
  __syncthreads();
}

__global__ void __launch_bounds__(256) k1(const u16* x, const u16* wq, const u16* w1, const u16* b1, float* q, float* h) {
  __shared__ float xs[D], xn[D], yn[D], red[8];
  for (int i = threadIdx.x; i < D; i += 256) xs[i] = bf2f(x[i]);
  __syncthreads();
  layernorm(xs, xn, red);
  layernorm(xn, yn, red);
  int col = blockIdx.x * 256 + threadIdx.x;
  if (col < HQ * DH) {
    float acc = 0.f;
    for (int i = 0; i < D; ++i) acc += xn[i] * bf2f(wq[(size_t)i * (HQ * DH) + col]);
    q[col] = rbf(acc);
  } else {
    col -= HQ * DH;
    if (col < F) {
      float acc = 0.f;
      for (int i = 0; i < D; ++i) acc += yn[i] * bf2f(w1[(size_t)i * F + col]);
      float t = rbf(rbf(acc) + bf2f(b1[col]));
      float g = 0.5f * t * (1.0f + tanhf(0.7978845608028654f * (t + 0.044715f * t * t * t)));
      h[col] = rbf(g);
    }
  }
}

__global__ void __launch_bounds__(256) k2(const float* q, const u16* k, const u16* v, float* pm, float* pl, float* po) {
  __shared__ float qs[DH], ps[PS], red[8];
  int s = blockIdx.x, hq = blockIdx.y, hk = hq / 2;
  if (threadIdx.x < DH) qs[threadIdx.x] = q[hq * DH + threadIdx.x];
  __syncthreads();
  int pos = SK - W + s * PS + threadIdx.x;
  const u16* kr = k + ((size_t)hk * SK + pos) * DH;
  float acc = 0.f;
  for (int d = 0; d < DH; ++d) acc += qs[d] * bf2f(kr[d]);
  float sc = acc * 0.08838834764831845f;
  float m = sc;
  for (int o = 32; o >= 1; o >>= 1) m = fmaxf(m, __shfl_xor(m, o));
  if ((threadIdx.x & 63) == 0) red[threadIdx.x >> 6] = m;
  __syncthreads();
  m = fmaxf(fmaxf(red[0], red[1]), fmaxf(red[2], red[3]));
  float p = __expf(sc - m);
  ps[threadIdx.x] = p;
  float l = block_sum(p, red);
  __syncthreads();
  if (threadIdx.x < DH) {
    float o = 0.f;
    const u16* vr = v + ((size_t)hk * SK + SK - W + s * PS) * DH + threadIdx.x;
    for (int i = 0; i < PS; ++i) o += ps[i] * bf2f(vr[(size_t)i * DH]);
    po[(hq * NSPLIT + s) * DH + threadIdx.x] = o;
  }
  if (threadIdx.x == 0) { pm[hq * NSPLIT + s] = m; pl[hq * NSPLIT + s] = l; }
}

__global__ void __launch_bounds__(128) k3(const float* pm, const float* pl, const float* po, float* o) {
  int hq = blockIdx.x;
  float M = -INFINITY;
  for (int s = 0; s < NSPLIT; ++s) M = fmaxf(M, pm[hq * NSPLIT + s]);
  float L = 0.f, acc = 0.f;
  for (int s = 0; s < NSPLIT; ++s) {
    float w = __expf(pm[hq * NSPLIT + s] - M);
    L += w * pl[hq * NSPLIT + s];
    acc += w * po[(hq * NSPLIT + s) * DH + threadIdx.x];
  }
  o[hq * DH + threadIdx.x] = rbf(acc / L);
}

__global__ void __launch_bounds__(1024) k4(const u16* x, const float* o, const float* h, const u16* wo, const u16* w2, const u16* b2, u16* out) {
  __shared__ float ra[16][64], rb[16][64];
  int c = blockIdx.x * 64 + (threadIdx.x & 63), g = threadIdx.x >> 6;
  float a = 0.f, b = 0.f;
  for (int r = g; r < HQ * DH; r += 16) a += o[r] * bf2f(wo[(size_t)r * D + c]);
  for (int r = g; r < F; r += 16) b += h[r] * bf2f(w2[(size_t)r * D + c]);
  ra[g][threadIdx.x & 63] = a; rb[g][threadIdx.x & 63] = b;
  __syncthreads();
  if (g == 0) {
    float A = 0.f, B = 0.f;
    for (int i = 0; i < 16; ++i) { A += ra[i][threadIdx.x]; B += rb[i][threadIdx.x]; }
    float mlp = rbf(rbf(B) + bf2f(b2[c]));
    float t = rbf(bf2f(x[c]) + rbf(A));
    out[c] = f2bf(t + mlp);
  }
}

extern "C" void kernel_launch(void* const* d_in, const int* in_sizes, int n_in, void* d_out, int out_size, void* d_ws, size_t ws_size, hipStream_t stream) {
  const u16* x = (const u16*)d_in[0];
  const u16* k = (const u16*)d_in[1];
  const u16* v = (const u16*)d_in[2];
  const u16* wq = (const u16*)d_in[3];
  const u16* wo = (const u16*)d_in[4];
  const u16* w1 = (const u16*)d_in[6];
  const u16* b1 = (const u16*)d_in[7];
  const u16* w2 = (const u16*)d_in[8];
  const u16* b2 = (const u16*)d_in[9];
  float* ws = (float*)d_ws;
  float* q = ws; float* h = ws + 4096; float* pm = ws + 16384; float* pl = ws + 16384 + 512; float* po = ws + 32768; float* o = ws + 32768 + 65536 * 2;
  k1<<<(HQ * DH + F) / 256, 256, 0, stream>>>(x, wq, w1, b1, q, h);
  k2<<<dim3(NSPLIT, HQ), 256, 0, stream>>>(q, k, v, pm, pl, po);
  k3<<<HQ, 128, 0, stream>>>(pm, pl, po, o);
  k4<<<D / 64, 1024, 0, stream>>>(x, o, h, wo, w2, b2, (u16*)d_out);
}
```

```cpp
#include <hip/hip_runtime.h>
#include <stdint.h>

typedef unsigned v4u __attribute__((ext_vector_type(4)));
typedef unsigned v2u __attribute__((ext_vector_type(2)));
typedef float v4f __attribute__((ext_vector_type(4)));
typedef __bf16 bf16x8 __attribute__((ext_vector_type(8)));
typedef __bf16 bf16x2 __attribute__((ext_vector_type(2)));
#define LAS __attribute__((address_space(3)))

constexpr int D = 576, SK = 16384, WIN = 4096, F = 4608;
constexpr int KV_BYTES = 16 * SK * 128 * 2;
constexpr int WQ_BYTES = D * 4096 * 2, W1_BYTES = D * F * 2;
#define NRED 36
constexpr int R = NRED, QPR = 144 / R;
constexpr int WS_Q = 4096, WS_H = WS_Q + 256 * 512, WS_O = WS_H + 72 * 5 * 512, O_STRIDE = 1152, WS_F = WS_O + 256 * O_STRIDE,
              WS_END = WS_F + 256 * 2304;
constexpr unsigned EMPTY = 0xFFFFFFFFu;
constexpr int OOB = 0x7FFFFFFF;
#define AUX_STREAM 2
constexpr int AUX_NT = AUX_STREAM, AUX_SC1 = 16;
constexpr int SPIN_G = 20000, SPIN_L = 400000;
constexpr float LOG2E = 1.4426950408889634f;

constexpr int L_FLAG = 0;
constexpr int FL_CLEAN = 0, FL_Q = 1, FL_H = 2, FL_VISS = 3, FL_ADONE = 4, FL_O = 5, FL_FPUB = 6, FL_RED = 7;
constexpr int L_XS = 16  , L_QS = L_XS + 4 * 768  , L_HS = L_QS + 128  , L_OS = L_HS + 32  ,
              L_ML = L_OS + 16  , L_ORED = L_ML + 16  , L_FRED = L_ORED + 1024  , L_END = L_FRED + 256;
#define LDS_PIN (24 * 1024)

#define RS(p, n) __builtin_amdgcn_make_buffer_rsrc((void*)(p), 0, (int)(n), 0x00020000)
#define SB() __builtin_amdgcn_sched_barrier(0)
#define CBAR() asm volatile("" ::: "memory")
#define LDS_DRAIN() asm volatile("s_waitcnt lgkmcnt(0)" ::: "memory")
#define VM_DRAIN() asm volatile("s_waitcnt vmcnt(0)" ::: "memory")

#define STAMP(i) do {} while (0)

__device__ __forceinline__ float asf(unsigned u) { return __builtin_bit_cast(float, u); }
__device__ __forceinline__ unsigned asu(float f) { return __builtin_bit_cast(unsigned, f); }
__device__ __forceinline__ unsigned cvtpk(float lo, float hi) {
  bf16x2 r = {(__bf16)lo, (__bf16)hi};
  return __builtin_bit_cast(unsigned, r);
}
__device__ __forceinline__ float rbf(float f) { return asf(cvtpk(f, f) & 0xFFFF0000u); }
template <int CTRL> __device__ __forceinline__ float dpp(float x) {
  return asf(__builtin_amdgcn_mov_dpp(asu(x), CTRL, 0xf, 0xf, true));
}
constexpr int XOR1 = 0xB1, XOR2 = 0x4E, XOR7 = 0x141, ROR8 = 0x128, ROR4 = 0x124;
__device__ __forceinline__ float xrow_sum(float x) {
  auto s = __builtin_amdgcn_permlane16_swap(asu(x), asu(x), false, false);
  x = asf(s[0]) + asf(s[1]);
  auto t = __builtin_amdgcn_permlane32_swap(asu(x), asu(x), false, false);
  return asf(t[0]) + asf(t[1]);
}
__device__ __forceinline__ float xrow_max(float x) {
  auto s = __builtin_amdgcn_permlane16_swap(asu(x), asu(x), false, false);
  x = fmaxf(asf(s[0]), asf(s[1]));
  auto t = __builtin_amdgcn_permlane32_swap(asu(x), asu(x), false, false);
  return fmaxf(asf(t[0]), asf(t[1]));
}
__device__ __forceinline__ float wave_sum(float x) {
  x += dpp<XOR1>(x); x += dpp<XOR2>(x); x += dpp<XOR7>(x); x += dpp<ROR8>(x);
  return xrow_sum(x);
}
__device__ __forceinline__ v4f mfma(v4u a, v4u b, v4f c) {
  return __builtin_amdgcn_mfma_f32_16x16x32_bf16(__builtin_bit_cast(bf16x8, a), __builtin_bit_cast(bf16x8, b), c, 0, 0, 0);
}
__device__ __forceinline__ float dot2(unsigned a, unsigned b, float c) {
  return __builtin_amdgcn_fdot2_f32_bf16(__builtin_bit_cast(bf16x2, a), __builtin_bit_cast(bf16x2, b), c, false);
}
__device__ __forceinline__ v4f fold(v4f d) {
  v4f r;
#pragma unroll
  for (int i = 0; i < 4; ++i) {
    const unsigned z = asu(dpp<ROR8>(d[i]));
    auto s = __builtin_amdgcn_permlane32_swap(z, z, false, false);
    r[i] = d[i] + asf(s[1]);
  }
  return r;
}
__device__ __forceinline__ v4u box(v4f v) {
  v4u r;
#pragma unroll
  for (int i = 0; i < 4; ++i) r[i] = min(asu(v[i]), 0xFFFFFFFEu);
  return r;
}
__device__ __forceinline__ bool full(v4u v) { return (v.x != EMPTY) & (v.y != EMPTY) & (v.z != EMPTY) & (v.w != EMPTY); }
__device__ __forceinline__ void lds_wait(volatile LAS unsigned* f, unsigned need) {
#pragma nounroll
  for (int sp = 0; sp < SPIN_L; ++sp) {
    if ((unsigned)__builtin_amdgcn_readfirstlane(*f) >= need) break;
    __builtin_amdgcn_s_sleep(1);
  }
  CBAR();
}
__device__ __forceinline__ void layernorm(float (&v)[10], int lane) {
  float s = 0.f;
#pragma unroll
  for (int i = 0; i < 10; ++i) s += v[i];
  const float mean = wave_sum(s) * (1.0f / D);
  float s2 = 0.f, s3 = 0.f;
#pragma unroll
  for (int i = 0; i < 8; ++i) { const float d = v[i] - mean; s2 = fmaf(d, d, s2); }
#pragma unroll
  for (int i = 8; i < 10; ++i) { const float d = v[i] - mean; s3 = fmaf(d, d, s3); }
  s2 += lane < 32 ? s3 : 0.f;
  const float var = wave_sum(s2) * (1.0f / D);
  const float mb = rbf(mean), sd = rbf(__builtin_sqrtf(rbf(rbf(var) + 1e-5f)));
  const float inv = 1.0f / sd;
#pragma unroll
  for (int i = 0; i < 10; ++i) v[i] = rbf(rbf(v[i] - mb) * inv);
  if (lane >= 32) { v[8] = 0.f; v[9] = 0.f; }
}

__global__ void __launch_bounds__(512, 2)
fused_decode(const void* __restrict__ x, const void* __restrict__ kc, const void* __restrict__ vc, const void* __restrict__ wq,
             const void* __restrict__ wo, const void* __restrict__ w1, const void* __restrict__ b1, const void* __restrict__ w2,
             const void* __restrict__ b2, void* __restrict__ out, void* __restrict__ ws) {
  __shared__ __attribute__((aligned(16))) unsigned smem[LDS_PIN];
  LAS unsigned* L = (LAS unsigned*)smem;
  volatile LAS unsigned* FL = (volatile LAS unsigned*)smem;
  const int tid = threadIdx.x, lane = tid & 63, wave = __builtin_amdgcn_readfirstlane(tid >> 6);
  const int b = blockIdx.x, j = (b & 7) * 32 + (b >> 3), h = j >> 4, s = j & 15;
  STAMP(0);
  if (tid < 16) L[L_FLAG + tid] = 0u;
  __syncthreads();
  const auto rws = RS(ws, WS_END);
  v4u msk;
#pragma unroll
  for (int v = 0; v < 4; ++v) msk[v] = (((lane & 7) >> 1) == v) ? ((lane & 1) ? 0xFFFF0000u : 0x0000FFFFu) : 0u;

  if (wave >= 4) {
    const int aw = wave - 4;
    const auto rk = RS(kc, KV_BYTES), rv = RS(vc, KV_BYTES);
    const int base = (h * SK + (SK - WIN) + s * 256) * 256 + aw * 1024, rot = s & 7;
    v4u kk[16], vv[16];
#pragma unroll
    for (int i = 0; i < 16; ++i) kk[i] = __builtin_amdgcn_raw_buffer_load_b128(rk, lane * 16, base + ((i + rot) & 15) * 4096, AUX_NT);
#pragma unroll
    for (int i = 0; i < 16; ++i) vv[i] = __builtin_amdgcn_raw_buffer_load_b128(rv, lane * 16, base + ((i + rot) & 15) * 4096, AUX_NT);
    SB();
    if (lane == 0) __hip_atomic_fetch_add(&L[L_FLAG + FL_VISS], 1u, __ATOMIC_RELAXED, __HIP_MEMORY_SCOPE_WORKGROUP);
    STAMP(1);
    lds_wait(FL + FL_Q, 1u);
    STAMP(2);
    const int c = lane & 15, hb = c >> 3;
    const v4u qa = *(LAS v4u*)(L + L_QS + hb * 64 + c * 4), qb = *(LAS v4u*)(L + L_QS + (1 - hb) * 64 + c * 4);
    float sc[16];
#pragma unroll
    for (int i = 0; i < 16; ++i) {
      float a = 0.f, bb = 0.f;
#pragma unroll
      for (int t = 0; t < 4; ++t) { a = dot2(kk[i][t], qa[t], a); bb = dot2(kk[i][t], qb[t], bb); }
      float u = a + dpp<ROR8>(bb);
      u += dpp<XOR1>(u); u += dpp<XOR2>(u); u += dpp<XOR7>(u);
      sc[i] = rbf(rbf(u) * 0.08838834764831845f);
    }
    float m = sc[0];
#pragma unroll
    for (int i = 1; i < 16; ++i) m = fmaxf(m, sc[i]);
    m = xrow_max(m);
    float l = 0.f;
    unsigned pk[16];
#pragma unroll
    for (int i = 0; i < 16; ++i) { const float p = __builtin_amdgcn_exp2f((sc[i] - m) * LOG2E); l += p; pk[i] = cvtpk(p, p); }
    l = xrow_sum(l);
    STAMP(3);
    v4f acc = {0.f, 0.f, 0.f, 0.f};
#pragma unroll
    for (int i = 0; i < 16; ++i) acc = mfma(msk & pk[i], vv[i], acc);
    *(LAS v4f*)(L + L_ORED + aw * 256 + lane * 4) = acc;
    if ((lane & 55) == 0) *(LAS v2u*)(L + L_ML + aw * 4 + hb * 2) = v2u{asu(m), asu(l)};
    LDS_DRAIN();
    if (lane == 0) __hip_atomic_fetch_add(&L[L_FLAG + FL_ADONE], 1u, __ATOMIC_RELAXED, __HIP_MEMORY_SCOPE_WORKGROUP);
    STAMP(4);
    if (b < R) {
      const int t = aw * 64 + lane;
      const v2u ep = __builtin_amdgcn_raw_buffer_load_b64(rws, 0, 0, AUX_SC1);
      const unsigned long long e1 = (((unsigned long long)ep.y << 32) | ep.x) + 1ull;
      const unsigned thi = (unsigned)e1, tlo = (unsigned)(e1 >> 32) & 0xFFu;
      const int qd = t % QPR, col = (b * QPR + qd) * 4;
      const v2u xb = __builtin_amdgcn_raw_buffer_load_b64(RS(x, D * 2), col * 2, 0, 0);
      const v2u bb = __builtin_amdgcn_raw_buffer_load_b64(RS(b2, D * 2), col * 2, 0, 0);
#pragma nounroll
      for (int sp = 0; sp < SPIN_G; ++sp) {
        CBAR();
        const v2u gq = __builtin_amdgcn_raw_buffer_load_b64(rws, WS_Q + t * 512, 0, AUX_SC1);
        if (__all(gq.y == thi && (gq.x & 0xFFu) == tlo)) break;
      }
      if (lane == 0) __hip_atomic_fetch_add(&L[L_FLAG + FL_RED], 1u, __ATOMIC_RELAXED, __HIP_MEMORY_SCOPE_WORKGROUP);
      lds_wait(FL + FL_RED, 4u);
      lds_wait(FL + FL_FPUB, 3u);
      STAMP(5);
      v4u pv[QPR];
#pragma unroll
      for (int i = 0; i < QPR; ++i) pv[i] = v4u{EMPTY, EMPTY, EMPTY, EMPTY};
#pragma nounroll
      for (int sp = 0; sp < SPIN_G; ++sp) {
        CBAR();
        bool ok = true;
#pragma unroll
        for (int i = 0; i < QPR; ++i) {
          const v4u n = __builtin_amdgcn_raw_buffer_load_b128(rws, full(pv[i]) ? OOB : WS_F + (b * 256 * QPR + t + i * 256) * 16, 0, AUX_SC1);
          pv[i] = full(pv[i]) ? pv[i] : n;
        }
#pragma unroll
        for (int i = 0; i < QPR; ++i) ok &= full(pv[i]);
        if (__all(ok)) break;
      }
      STAMP(6);
      v4f sum = __builtin_bit_cast(v4f, pv[0]);
#pragma unroll
      for (int i = 1; i < QPR; ++i) sum += __builtin_bit_cast(v4f, pv[i]);
#pragma unroll
      for (int i = 0; i < 4; ++i) {
        float u = sum[i];
        if (QPR <= 1) u += dpp<XOR1>(u);
        if (QPR <= 2) u += dpp<XOR2>(u);
        u += dpp<ROR8>(u);
        if (QPR <= 4) u += dpp<ROR4>(u);
        sum[i] = xrow_sum(u);
      }
      if (lane < QPR) *(LAS v4f*)(L + L_FRED + aw * 64 + lane * 4) = sum;
      LDS_DRAIN();
      unsigned old = 0;
      if (lane == 0) old = __hip_atomic_fetch_add(&L[L_FLAG + FL_RED], 1u, __ATOMIC_RELAXED, __HIP_MEMORY_SCOPE_WORKGROUP);
      if (__builtin_amdgcn_readfirstlane(old) == 7u) {
        CBAR();
        if (lane < QPR) {
          v4f tot = *(LAS v4f*)(L + L_FRED + lane * 4);
#pragma unroll
          for (int w = 1; w < 4; ++w) tot += *(LAS v4f*)(L + L_FRED + w * 64 + lane * 4);
          const float r0 = tot[0] + asf(xb.x << 16) + asf(bb.x << 16), r1 = tot[1] + asf(xb.x & 0xFFFF0000u) + asf(bb.x & 0xFFFF0000u);
          const float r2 = tot[2] + asf(xb.y << 16) + asf(bb.y << 16), r3 = tot[3] + asf(xb.y & 0xFFFF0000u) + asf(bb.y & 0xFFFF0000u);
          __builtin_amdgcn_raw_buffer_store_b64(v2u{cvtpk(r0, r1), cvtpk(r2, r3)}, RS(out, D * 2), col * 2, 0, 0);
          if (b == 0 && lane == 0) __builtin_amdgcn_raw_buffer_store_b64(v2u{(unsigned)e1, (unsigned)(e1 >> 32)}, rws, 0, 0, AUX_SC1);
        }
      }
      STAMP(7);
    }
  } else {
    __builtin_amdgcn_s_setprio(3);
    if (wave != 2) {
      const int r8 = lane >> 3, c8 = lane & 7;
      const int lcA = (j * 9) >> 5, rowA = 18 * ((j * 9) & 31), nA = min(162, D - rowA);
      const int stride = wave == 0 ? 8192 : 9216;
      const int row0 = wave == 0 ? (s >> 2) * 144 : (wave == 1 ? rowA : 0);
      const int n = wave == 0 ? 144 : (wave == 1 ? nA : 162 - nA);
      const int colb = wave == 0 ? h * 512 + (s & 3) * 128 : (wave == 1 ? lcA : lcA + 1) * 128;
      const int gout = wave == 0 ? WS_Q + j * 512 : (wave == 1 ? WS_H + (lcA * 5 + j - (32 * lcA) / 9) * 512 : WS_H + (lcA + 1) * 5 * 512);
      if (n > 0) {
        const auto rw = RS((const char*)(wave == 0 ? wq : w1) + row0 * stride + colb, (n - 1) * stride + 128);
        const auto rx = RS(x, D * 2);
        unsigned xr[5];
#pragma unroll
        for (int i = 0; i < 5; ++i) xr[i] = __builtin_amdgcn_raw_buffer_load_b32(rx, lane * 4 + i * 256, 0, 0);
        const v2u ep = __builtin_amdgcn_raw_buffer_load_b64(rws, 0, 0, AUX_SC1);
        v4u w[21];
        int vo = r8 * stride + c8 * 16;
#pragma unroll
        for (int i = 0; i < 21; ++i) { w[i] = __builtin_amdgcn_raw_buffer_load_b128(rw, vo, 0, AUX_NT); vo += 8 * stride; }
        const unsigned short bias = __builtin_amdgcn_raw_buffer_load_b16(RS(b1, F * 2), lane < 18 ? lane * 2 + j * 36 : OOB, 0, 0);
        SB();
        STAMP(1);
        float xv[10];
#pragma unroll
        for (int i = 0; i < 5; ++i) { xv[2 * i] = asf(xr[i] << 16); xv[2 * i + 1] = asf(xr[i] & 0xFFFF0000u); }
#pragma nounroll
        for (int rep = wave == 0 ? 1 : 0; rep < 2; ++rep) layernorm(xv, lane);
        LAS unsigned* XS = L + L_XS + wave * 768;
#pragma unroll
        for (int i = 0; i < 5; ++i)
          *(LAS v2u*)(XS + i * 128 + lane * 2) = v2u{asu(xv[2 * i]) | (asu(xv[2 * i]) >> 16), asu(xv[2 * i + 1]) | (asu(xv[2 * i + 1]) >> 16)};
        *(LAS v2u*)(XS + 640 + lane * 2) = v2u{0u, 0u};
        LDS_DRAIN();
        v4f acc = {0.f, 0.f, 0.f, 0.f};
#pragma unroll
        for (int i = 0; i < 21; ++i) acc = mfma(msk & XS[row0 + i * 8 + r8], w[i], acc);
        acc = fold(acc);
        const unsigned long long e1 = (((unsigned long long)ep.y << 32) | ep.x) + 1ull;
        const unsigned thi = (unsigned)e1, tlo = (unsigned)(e1 >> 32) & 0xFFu;
        STAMP(2);
        lds_wait(FL + FL_CLEAN, 1u);
        if ((lane & 40) == 0) {
#pragma unroll
          for (int i = 0; i < 4; ++i)
            __builtin_amdgcn_raw_buffer_store_b64(v2u{(asu(acc[i]) & 0xFFFFFF00u) | tlo, thi}, rws, (c8 * 8 + (lane >> 4) * 4 + i) * 8, gout, AUX_SC1);
        }
        STAMP(3);
        if (wave == 0) {
          v2u g[16];
#pragma nounroll
          for (int sp = 0; sp < SPIN_G; ++sp) {
            CBAR();
            bool ok = true;
#pragma unroll
            for (int i = 0; i < 16; ++i) g[i] = __builtin_amdgcn_raw_buffer_load_b64(rws, lane * 8 + (i & 7) * 512, WS_Q + h * 8192 + (i >> 3) * 4096, AUX_SC1);
#pragma unroll
            for (int i = 0; i < 16; ++i) ok &= (g[i].y == thi) & ((g[i].x & 0xFFu) == tlo);
            if (__all(ok)) break;
          }
#pragma unroll
          for (int cg = 0; cg < 4; ++cg) {
            const float q = (asf(g[cg].x & 0xFFFFFF00u) + asf(g[4 + cg].x & 0xFFFFFF00u)) + (asf(g[8 + cg].x & 0xFFFFFF00u) + asf(g[12 + cg].x & 0xFFFFFF00u));
            ((LAS unsigned short*)(L + L_QS))[cg * 64 + lane] = (unsigned short)cvtpk(q, q);
          }
          LDS_DRAIN();
          FL[FL_Q] = 1u;
        } else if (wave == 1) {
          const int f = j * 18 + lane, lc = f >> 6;
          const int cnt = lane < 18 ? (576 * lc + 575) / 162 - (32 * lc) / 9 + 1 : 0;
          v2u g[5];
#pragma nounroll
          for (int sp = 0; sp < SPIN_G; ++sp) {
            CBAR();
            bool ok = true;
#pragma unroll
            for (int k = 0; k < 5; ++k) g[k] = __builtin_amdgcn_raw_buffer_load_b64(rws, k < cnt ? WS_H + (lc * 5 + k) * 512 + (f & 63) * 8 : OOB, 0, AUX_SC1);
#pragma unroll
            for (int k = 0; k < 5; ++k) ok &= (k >= cnt) | ((g[k].y == thi) & ((g[k].x & 0xFFu) == tlo));
            if (__all(ok)) break;
          }
          float hsum = 0.f;
#pragma unroll
          for (int k = 0; k < 5; ++k) hsum += asf(g[k].x & 0xFFFFFF00u);
          const float t = rbf(rbf(hsum) + asf((unsigned)bias << 16));
          const float z2 = 2.0f * 0.7978845608028654f * (t + 0.044715f * t * t * t);
          const float ge = t / (1.0f + __builtin_amdgcn_exp2f(-z2 * LOG2E));
          if (lane < 24) L[L_HS + lane] = lane < 18 ? cvtpk(ge, ge) : 0u;
          LDS_DRAIN();
          FL[FL_H] = 1u;
        }
        STAMP(4);
      }
    }
    if (wave == 2) {
      const v4u e4 = {EMPTY, EMPTY, EMPTY, EMPTY};
      __builtin_amdgcn_raw_buffer_store_b128(e4, rws, WS_O + lane * 16, j * O_STRIDE, AUX_SC1);
      if (lane == 0) __builtin_amdgcn_raw_buffer_store_b128(e4, rws, WS_O + 1024, j * O_STRIDE, AUX_SC1);
#pragma unroll
      for (int i = 0; i < 3; ++i) {
        const int q = i * 48 + lane;
        if (lane < 48) __builtin_amdgcn_raw_buffer_store_b128(e4, rws, WS_F + (((q / QPR) * 256 + j) * QPR + q % QPR) * 16, 0, AUX_SC1);
      }
      VM_DRAIN();
      FL[FL_CLEAN] = 1u;
      STAMP(1);
      lds_wait(FL + FL_ADONE, 4u);
      STAMP(2);
      {
        const int hh = lane >> 5;
        v2u ml[4];
        v4f d[4];
#pragma unroll
        for (int w = 0; w < 4; ++w) { ml[w] = *(LAS v2u*)(L + L_ML + w * 4 + hh * 2); d[w] = *(LAS v4f*)(L + L_ORED + w * 256 + lane * 4); }
        const float M = fmaxf(fmaxf(asf(ml[0].x), asf(ml[1].x)), fmaxf(asf(ml[2].x), asf(ml[3].x)));
        v4f o = {0.f, 0.f, 0.f, 0.f};
        float Ls = 0.f;
#pragma unroll
        for (int w = 0; w < 4; ++w) { const float fw = __builtin_amdgcn_exp2f((asf(ml[w].x) - M) * LOG2E); o += d[w] * fw; Ls = fmaf(asf(ml[w].y), fw, Ls); }
        __builtin_amdgcn_raw_buffer_store_b128(box(o), rws, WS_O + (hh * 128 + (lane & 15) * 8 + ((lane >> 4) & 1) * 4) * 4, j * O_STRIDE, AUX_SC1);
        if ((lane & 31) == 0)
          __builtin_amdgcn_raw_buffer_store_b64(v2u{min(asu(M), 0xFFFFFFFEu), min(asu(Ls), 0xFFFFFFFEu)}, rws, WS_O + 1024 + hh * 8, j * O_STRIDE, AUX_SC1);
      }
      STAMP(3);
      v4u ov;
      v2u ml;
#pragma nounroll
      for (int sp = 0; sp < SPIN_G; ++sp) {
        CBAR();
        ov = __builtin_amdgcn_raw_buffer_load_b128(rws, WS_O + (lane >> 2) * O_STRIDE + (lane & 3) * 16, h * 16 * O_STRIDE + s * 64, AUX_SC1);
        ml = __builtin_amdgcn_raw_buffer_load_b64(rws, WS_O + 1024 + (lane >> 2) * O_STRIDE, h * 16 * O_STRIDE + (s >> 3) * 8, AUX_SC1);
        if (__all(full(ov) & (ml.x != EMPTY) & (ml.y != EMPTY))) break;
      }
      STAMP(4);
      float M = asf(ml.x);
      M = fmaxf(M, dpp<ROR8>(M)); M = fmaxf(M, dpp<ROR4>(M)); M = xrow_max(M);
      const float fw = __builtin_amdgcn_exp2f((asf(ml.x) - M) * LOG2E);
      float Ls = asf(ml.y) * fw;
      Ls += dpp<ROR8>(Ls); Ls += dpp<ROR4>(Ls); Ls = xrow_sum(Ls);
      const float inv = 1.0f / Ls;
      v4u op;
#pragma unroll
      for (int i = 0; i < 4; ++i) {
        float u = asf(ov[i]) * fw;
        u += dpp<ROR8>(u); u += dpp<ROR4>(u); u = xrow_sum(u) * inv;
        op[i] = cvtpk(u, u);
      }
      if (lane < 4) *(LAS v4u*)(L + L_OS + lane * 4) = op;
      LDS_DRAIN();
      FL[FL_O] = 1u;
      STAMP(5);
    } else {
      const int fs = wave == 3 ? 2 : wave, r8 = lane >> 3, c8 = lane & 7;
      const auto ro = RS(wo, WQ_BYTES), r2 = RS(w2, W1_BYTES);
      lds_wait(FL + FL_VISS, 4u);
      v4u wo_[2][3], w2_[3][3];
      const int vo = r8 * 1152 + c8 * 16;
#pragma unroll
      for (int rg = 0; rg < 2; ++rg)
#pragma unroll
        for (int k = 0; k < 3; ++k) wo_[rg][k] = __builtin_amdgcn_raw_buffer_load_b128(ro, vo + k * 128, j * 18432 + rg * 9216 + fs * 384, AUX_NT);
#pragma unroll
      for (int rg = 0; rg < 3; ++rg)
#pragma unroll
        for (int k = 0; k < 3; ++k)
          w2_[rg][k] = __builtin_amdgcn_raw_buffer_load_b128(r2, (rg < 2 || r8 < 2) ? vo + k * 128 : OOB, j * 20736 + rg * 9216 + fs * 384, AUX_NT);
      SB();
      STAMP(5);
      lds_wait(FL + FL_H, 1u);
      v4f acc[3];
#pragma unroll
      for (int k = 0; k < 3; ++k) acc[k] = v4f{0.f, 0.f, 0.f, 0.f};
#pragma unroll
      for (int rg = 0; rg < 3; ++rg) {
        const v4u a = msk & L[L_HS + rg * 8 + r8];
#pragma unroll
        for (int k = 0; k < 3; ++k) acc[k] = mfma(a, w2_[rg][k], acc[k]);
      }
      asm volatile("" : "+v"(acc[0]), "+v"(acc[1]), "+v"(acc[2]));
      STAMP(6);
      lds_wait(FL + FL_O, 1u);
#pragma unroll
      for (int rg = 0; rg < 2; ++rg) {
        const v4u a = msk & L[L_OS + rg * 8 + r8];
#pragma unroll
        for (int k = 0; k < 3; ++k) acc[k] = mfma(a, wo_[rg][k], acc[k]);
      }
#pragma unroll
      for (int k = 0; k < 3; ++k) {
        const v4u r = box(fold(acc[k]));
        const int q = (fs * 3 + k) * 16 + c8 * 2 + (lane >> 4);
        if ((lane & 40) == 0) __builtin_amdgcn_raw_buffer_store_b128(r, rws, WS_F + (((q / QPR) * 256 + j) * QPR + q % QPR) * 16, 0, AUX_SC1);
      }
      if (lane == 0) __hip_atomic_fetch_add(&L[L_FLAG + FL_FPUB], 1u, __ATOMIC_RELAXED, __HIP_MEMORY_SCOPE_WORKGROUP);
      STAMP(7);
    }
  }
}

extern "C" void kernel_launch(void* const* d_in, const int* in_sizes, int n_in, void* d_out, int out_size, void* d_ws, size_t ws_size,
                              hipStream_t stream) {
  fused_decode<<<256, 512, 0, stream>>>(d_in[0], d_in[1], d_in[2], d_in[3], d_in[4], d_in[6], d_in[7], d_in[8], d_in[9], d_out, d_ws);
}
```

```cpp
#include <hip/hip_runtime.h>
#include <stdint.h>

typedef unsigned v4u __attribute__((ext_vector_type(4)));
typedef unsigned v2u __attribute__((ext_vector_type(2)));
typedef float v4f __attribute__((ext_vector_type(4)));
typedef __bf16 bf16x8 __attribute__((ext_vector_type(8)));
typedef __bf16 bf16x2 __attribute__((ext_vector_type(2)));
#define LAS __attribute__((address_space(3)))

constexpr int D = 576, SK = 16384, WIN = 4096, F = 4608;
constexpr int KV_BYTES = 16 * SK * 128 * 2;
constexpr int WQ_BYTES = D * 4096 * 2, W1_BYTES = D * F * 2;
constexpr int R = 72;
constexpr int WS_CF = 1024  , WS_Q = 4096, WS_H = WS_Q + 256 * 512, WS_O = WS_H + 72 * 5 * 512, O_STRIDE = 1152,
              WS_F = WS_O + 256 * O_STRIDE, WS_END = WS_F + 256 * 2304;
constexpr unsigned EMPTY = 0xFFFFFFFFu;
constexpr int OOB = 0x7FFFFFFF;
#define AUX_STREAM 2
#define WIN_KV 7
#define HANDOVER 3
#define QDELAY 20
constexpr int AUX_NT = AUX_STREAM, AUX_SC1 = 16;
constexpr int SPIN_G = 20000, SPIN_L = 400000;
constexpr float LOG2E = 1.4426950408889634f;

constexpr int L_FLAG = 0;
constexpr int FL_Q = 0, FL_H = 1, FL_KISS = 2  , FL_VISS = 6, FL_P = 7  , FL_KDONE = 11, FL_FW = 12, FL_G = 13, FL_VDONE = 14,
              FL_OPUB = 15, FL_FPUB = 16;
constexpr int L_XS = 32  , L_QS = L_XS + 3 * 768  , L_HS = L_QS + 128  , L_ML = L_HS + 32  ,
              L_FW = L_ML + 16  , L_G = L_FW + 16  , L_ORED = L_G + 16  , L_P = L_ORED + 1024  ,
              L_END = L_P + 4096;
#define LDS_PIN (24 * 1024)

#define RS(p, n) __builtin_amdgcn_make_buffer_rsrc((void*)(p), 0, (int)(n), 0x00020000)
#define SB() __builtin_amdgcn_sched_barrier(0)
#define CBAR() asm volatile("" ::: "memory")
#define LDS_DRAIN() asm volatile("s_waitcnt lgkmcnt(0)" ::: "memory")
#define VM_DRAIN() asm volatile("s_waitcnt vmcnt(0)" ::: "memory")

#define STAMP(i) do {} while (0)

__device__ __forceinline__ float asf(unsigned u) { return __builtin_bit_cast(float, u); }
__device__ __forceinline__ unsigned asu(float f) { return __builtin_bit_cast(unsigned, f); }
__device__ __forceinline__ unsigned cvtpk(float lo, float hi) {
  bf16x2 r = {(__bf16)lo, (__bf16)hi};
  return __builtin_bit_cast(unsigned, r);
}
__device__ __forceinline__ float rbf(float f) { return asf(cvtpk(f, f) & 0xFFFF0000u); }
template <int CTRL> __device__ __forceinline__ float dpp(float x) {
  return asf(__builtin_amdgcn_mov_dpp(asu(x), CTRL, 0xf, 0xf, true));
}
constexpr int XOR1 = 0xB1, XOR2 = 0x4E, XOR7 = 0x141, ROR8 = 0x128, ROR4 = 0x124;
__device__ __forceinline__ float xrow_sum(float x) {
  auto s = __builtin_amdgcn_permlane16_swap(asu(x), asu(x), false, false);
  x = asf(s[0]) + asf(s[1]);
  auto t = __builtin_amdgcn_permlane32_swap(asu(x), asu(x), false, false);
  return asf(t[0]) + asf(t[1]);
}
__device__ __forceinline__ float xrow_max(float x) {
  auto s = __builtin_amdgcn_permlane16_swap(asu(x), asu(x), false, false);
  x = fmaxf(asf(s[0]), asf(s[1]));
  auto t = __builtin_amdgcn_permlane32_swap(asu(x), asu(x), false, false);
  return fmaxf(asf(t[0]), asf(t[1]));
}
__device__ __forceinline__ float wave_sum(float x) {
  x += dpp<XOR1>(x); x += dpp<XOR2>(x); x += dpp<XOR7>(x); x += dpp<ROR8>(x);
  return xrow_sum(x);
}
__device__ __forceinline__ v4f mfma(v4u a, v4u b, v4f c) {
  return __builtin_amdgcn_mfma_f32_16x16x32_bf16(__builtin_bit_cast(bf16x8, a), __builtin_bit_cast(bf16x8, b), c, 0, 0, 0);
}
__device__ __forceinline__ float dot2(unsigned a, unsigned b, float c) {
  return __builtin_amdgcn_fdot2_f32_bf16(__builtin_bit_cast(bf16x2, a), __builtin_bit_cast(bf16x2, b), c, false);
}
__device__ __forceinline__ v4f fold(v4f d) {
  v4f r;
#pragma unroll
  for (int i = 0; i < 4; ++i) {
    const unsigned z = asu(dpp<ROR8>(d[i]));
    auto s = __builtin_amdgcn_permlane32_swap(z, z, false, false);
    r[i] = d[i] + asf(s[1]);
  }
  return r;
}
__device__ __forceinline__ v4u box(v4f v) {
  v4u r;
#pragma unroll
  for (int i = 0; i < 4; ++i) r[i] = min(asu(v[i]), 0xFFFFFFFEu);
  return r;
}
__device__ __forceinline__ bool full(v4u v) { return (v.x != EMPTY) & (v.y != EMPTY) & (v.z != EMPTY) & (v.w != EMPTY); }
__device__ __forceinline__ void lds_wait(volatile LAS unsigned* f, unsigned need) {
#pragma nounroll
  for (int sp = 0; sp < SPIN_L; ++sp) {
    if ((unsigned)__builtin_amdgcn_readfirstlane(*f) >= need) break;
    __builtin_amdgcn_s_sleep(1);
  }
  CBAR();
}
__device__ __forceinline__ void layernorm(float (&v)[10], int lane) {
  float s = 0.f;
#pragma unroll
  for (int i = 0; i < 10; ++i) s += v[i];
  const float mean = wave_sum(s) * (1.0f / D);
  float s2 = 0.f, s3 = 0.f;
#pragma unroll
  for (int i = 0; i < 8; ++i) { const float d = v[i] - mean; s2 = fmaf(d, d, s2); }
#pragma unroll
  for (int i = 8; i < 10; ++i) { const float d = v[i] - mean; s3 = fmaf(d, d, s3); }
  s2 += lane < 32 ? s3 : 0.f;
  const float var = wave_sum(s2) * (1.0f / D);
  const float mb = rbf(mean), sd = rbf(__builtin_sqrtf(rbf(rbf(var) + 1e-5f)));
  const float inv = 1.0f / sd;
#pragma unroll
  for (int i = 0; i < 10; ++i) v[i] = rbf(rbf(v[i] - mb) * inv);
  if (lane >= 32) { v[8] = 0.f; v[9] = 0.f; }
}

template <int N> __device__ __forceinline__ void vmwait() { asm volatile("s_waitcnt vmcnt(%0)" ::"i"(N) : "memory"); }
__device__ __forceinline__ void lds_spin(volatile LAS unsigned* f, unsigned need) {
#pragma nounroll
  for (int sp = 0; sp < SPIN_L; ++sp)
    if ((unsigned)__builtin_amdgcn_readfirstlane(*f) >= need) break;
  CBAR();
}
__device__ __forceinline__ void lds_inc(LAS unsigned* f, int lane) {
  if (lane == 0) __hip_atomic_fetch_add(f, 1u, __ATOMIC_RELAXED, __HIP_MEMORY_SCOPE_WORKGROUP);
}
__device__ __forceinline__ float row_sum(float x) {
  x += dpp<XOR1>(x); x += dpp<XOR2>(x); x += dpp<XOR7>(x); x += dpp<ROR8>(x);
  return x;
}
__device__ __forceinline__ float row_max(float x) {
  x = fmaxf(x, dpp<XOR1>(x)); x = fmaxf(x, dpp<XOR2>(x)); x = fmaxf(x, dpp<XOR7>(x)); x = fmaxf(x, dpp<ROR8>(x));
  return x;
}

constexpr int W_P0 = 0, W_K = 1, W_P1 = 5, W_P2 = 6, W_C = 7, W_V = 8, W_F = 12;

__global__ void __launch_bounds__(1024, 4)
fused_decode(const void* __restrict__ x, const void* __restrict__ kc, const void* __restrict__ vc, const void* __restrict__ wq,
             const void* __restrict__ wo, const void* __restrict__ w1, const void* __restrict__ b1, const void* __restrict__ w2,
             const void* __restrict__ b2, void* __restrict__ out, void* __restrict__ ws) {
  __shared__ __attribute__((aligned(16))) unsigned smem[LDS_PIN];
  LAS unsigned* L = (LAS unsigned*)smem;
  volatile LAS unsigned* FL = (volatile LAS unsigned*)smem;
  const int tid = threadIdx.x, lane = tid & 63, wave = __builtin_amdgcn_readfirstlane(tid >> 6);
  const int b = blockIdx.x, j = (b & 7) * 32 + (b >> 3), h = j >> 4, s = j & 15;
  STAMP(0);
  const unsigned long long t_begin = __builtin_amdgcn_s_memrealtime();
  if (tid < 32) L[L_FLAG + tid] = 0u;
  __syncthreads();
  const auto rws = RS(ws, WS_END);
  const int kvbase = (h * SK + (SK - WIN) + s * 256) * 256, rot = s & 7;

  if (wave >= W_K && wave < W_K + 4) {
    const int aw = wave - W_K;
    const auto rk = RS(kc, KV_BYTES);
    v4u kk[16];
#pragma unroll
    for (int i = 0; i < 16; ++i) {
      if (i >= WIN_KV) vmwait<WIN_KV - 1>();
      kk[i] = __builtin_amdgcn_raw_buffer_load_b128(rk, lane * 16, kvbase + aw * 1024 + ((i + rot) & 15) * 4096, AUX_NT);
      SB();
    }
    vmwait<HANDOVER>();
    FL[FL_KISS + aw] = 1u;
    STAMP(1);
    const bool reducer = aw == 1 && (j & 31) < 9;
    const int red = (j >> 5) * 9 + (j & 31);
    unsigned long long e1 = 0;
    unsigned xb = 0, bb = 0;
    const int ocol = red * 8 + (lane & 1) * 4 + (lane >> 4);
    if (aw == 0 || reducer) {
      const v2u ep = __builtin_amdgcn_raw_buffer_load_b64(rws, 0, 0, AUX_SC1);
      e1 = (((unsigned long long)ep.y << 32) | ep.x) + 1ull;
      xb = __builtin_amdgcn_raw_buffer_load_b16(RS(x, D * 2), ocol * 2, 0, 0);
      bb = __builtin_amdgcn_raw_buffer_load_b16(RS(b2, D * 2), ocol * 2, 0, 0);
#pragma nounroll
      for (int sp = 0; sp < SPIN_G; ++sp) {
        CBAR();
        bool ok = true;
#pragma unroll
        for (int i = 0; i < 4; ++i) {
          const v2u cf = __builtin_amdgcn_raw_buffer_load_b64(rws, WS_CF + (aw == 0 ? h * 128 + (lane & 15) * 8 : lane * 8 + i * 512), 0, AUX_SC1);
          ok &= (cf.x == (unsigned)e1) & (cf.y == (unsigned)(e1 >> 32));
        }
        if (__all(ok)) break;
      }
    }
    lds_wait(FL + FL_Q, 1u);
    STAMP(2);
    const int c = lane & 15, hb = c >> 3;
    const v4u qa = *(LAS v4u*)(L + L_QS + hb * 64 + c * 4), qb = *(LAS v4u*)(L + L_QS + (1 - hb) * 64 + c * 4);
    float sc[16];
#pragma unroll
    for (int i = 0; i < 16; ++i) {
      float a = 0.f, bq = 0.f;
#pragma unroll
      for (int t = 0; t < 4; ++t) { a = dot2(kk[i][t], qa[t], a); bq = dot2(kk[i][t], qb[t], bq); }
      float u = a + dpp<ROR8>(bq);
      u += dpp<XOR1>(u); u += dpp<XOR2>(u); u += dpp<XOR7>(u);
      sc[i] = rbf(rbf(u) * 0.08838834764831845f);
    }
    float m = sc[0];
#pragma unroll
    for (int i = 1; i < 16; ++i) m = fmaxf(m, sc[i]);
    m = xrow_max(m);
    float l = 0.f;
#pragma unroll
    for (int i4 = 0; i4 < 4; ++i4) {
      v4u pk;
#pragma unroll
      for (int t = 0; t < 4; ++t) { const float p = __builtin_amdgcn_exp2f((sc[i4 * 4 + t] - m) * LOG2E); l += p; pk[t] = cvtpk(p, p); }
      *(LAS v4u*)(L + L_P + aw * 1024 + i4 * 256 + lane * 4) = pk;
    }
    l = xrow_sum(l);
    if ((lane & 55) == 0) *(LAS v2u*)(L + L_ML + aw * 4 + hb * 2) = v2u{asu(m), asu(l)};
    LDS_DRAIN();
    FL[FL_P + aw] = 1u;
    lds_inc(L + FL_KDONE, lane);
    STAMP(3);
    if (aw == 0) {
      lds_wait(FL + FL_KDONE, 4u);
      const int hh = lane >> 5, w = lane & 3;
      const v2u ml = *(LAS v2u*)(L + L_ML + w * 4 + hh * 2);
      float M = asf(ml.x);
      M = fmaxf(M, dpp<XOR1>(M)); M = fmaxf(M, dpp<XOR2>(M));
      const float fw = __builtin_amdgcn_exp2f((asf(ml.x) - M) * LOG2E);
      float Ls = asf(ml.y) * fw;
      Ls += dpp<XOR1>(Ls); Ls += dpp<XOR2>(Ls);
      if ((lane & 28) == 0) L[L_FW + hh * 4 + w] = asu(fw);
      if ((lane & 31) == 0)
        __builtin_amdgcn_raw_buffer_store_b64(v2u{min(asu(M), 0xFFFFFFFEu), min(asu(Ls), 0xFFFFFFFEu)}, rws, WS_O + 1024 + hh * 8, j * O_STRIDE, AUX_SC1);
      LDS_DRAIN();
      FL[FL_FW] = 1u;
      STAMP(4);
      v2u g;
#pragma nounroll
      for (int sp = 0; sp < SPIN_G; ++sp) {
        CBAR();
        g = __builtin_amdgcn_raw_buffer_load_b64(rws, WS_O + 1024 + (lane & 15) * O_STRIDE, h * 16 * O_STRIDE + (s >> 3) * 8, AUX_SC1);
        if (__all((g.x != EMPTY) & (g.y != EMPTY))) break;
      }
      const float Mg = row_max(asf(g.x));
      const float gw = __builtin_amdgcn_exp2f((asf(g.x) - Mg) * LOG2E);
      const float Lg = row_sum(asf(g.y) * gw);
      if (lane < 16) L[L_G + lane] = asu(gw / Lg);
      LDS_DRAIN();
      FL[FL_G] = 1u;
      STAMP(5);
    } else if (reducer) {
      lds_wait(FL + FL_FPUB, 5u);
      STAMP(4);
      v4f sum;
      bool done = false;
#pragma nounroll
      for (int sp = 0; sp < SPIN_G; ++sp) {
        CBAR();
        if (!done) {
          v4u pv[8];
#pragma unroll
          for (int i = 0; i < 8; ++i) pv[i] = __builtin_amdgcn_raw_buffer_load_b128(rws, lane * 16 + (i & 3) * 1024, WS_F + red * 8192 + (i >> 2) * 4096, AUX_SC1);
          unsigned mx = 0;
          sum = __builtin_bit_cast(v4f, pv[0]);
#pragma unroll
          for (int i = 1; i < 8; ++i) sum += __builtin_bit_cast(v4f, pv[i]);
          done = true;
          const float chk = (sum[0] + sum[1]) + (sum[2] + sum[3]);
          if (chk != chk) {
#pragma unroll
            for (int i = 0; i < 8; ++i) mx = max(max(mx, max(pv[i].x, pv[i].y)), max(pv[i].z, pv[i].w));
            done = mx != EMPTY;
          }
        }
        if (__all(done)) break;
      }
      STAMP(5);
      auto s02 = __builtin_amdgcn_permlane32_swap(asu(sum[0]), asu(sum[2]), false, false);
      auto s13 = __builtin_amdgcn_permlane32_swap(asu(sum[1]), asu(sum[3]), false, false);
      const float u0 = asf(s02[0]) + asf(s02[1]), u1 = asf(s13[0]) + asf(s13[1]);
      auto s01 = __builtin_amdgcn_permlane16_swap(asu(u0), asu(u1), false, false);
      float u = asf(s01[0]) + asf(s01[1]);
      u += dpp<ROR8>(u); u += dpp<ROR4>(u); u += dpp<XOR2>(u);
      u += asf(xb << 16) + asf(bb << 16);
      if ((lane & 14) == 0) __builtin_amdgcn_raw_buffer_store_b16((unsigned short)cvtpk(u, u), RS(out, D * 2), ocol * 2, 0, 0);
      if (j == 0 && lane == 0) __builtin_amdgcn_raw_buffer_store_b64(v2u{(unsigned)e1, (unsigned)(e1 >> 32)}, rws, 0, 0, AUX_SC1);
      STAMP(6);
    }
  } else if (wave >= W_V && wave < W_V + 4) {
    const int aw = wave - W_V;
    const auto rv = RS(vc, KV_BYTES);
    v4u msk;
#pragma unroll
    for (int v = 0; v < 4; ++v) msk[v] = (((lane & 7) >> 1) == v) ? ((lane & 1) ? 0xFFFF0000u : 0x0000FFFFu) : 0u;
    lds_wait(FL + FL_KISS + aw, 1u);
    STAMP(1);
    v4u vv[16];
#pragma unroll
    for (int i = 0; i < 16; ++i) {
      if (i >= WIN_KV) vmwait<WIN_KV - 1>();
      vv[i] = __builtin_amdgcn_raw_buffer_load_b128(rv, lane * 16, kvbase + aw * 1024 + ((i + rot) & 15) * 4096, AUX_NT);
      SB();
    }
    vmwait<HANDOVER>();
    lds_inc(L + FL_VISS, lane);
    STAMP(2);
    lds_wait(FL + FL_P + aw, 1u);
    STAMP(3);
    v4u pk[4];
#pragma unroll
    for (int i4 = 0; i4 < 4; ++i4) pk[i4] = *(LAS v4u*)(L + L_P + aw * 1024 + i4 * 256 + lane * 4);
    v4f acc = {0.f, 0.f, 0.f, 0.f};
#pragma unroll
    for (int i = 0; i < 16; ++i) acc = mfma(msk & pk[i >> 2][i & 3], vv[i], acc);
    *(LAS v4f*)(L + L_ORED + aw * 256 + lane * 4) = acc;
    LDS_DRAIN();
    lds_inc(L + FL_VDONE, lane);
    STAMP(4);
  } else if (wave == W_C) {
    const v2u ep = __builtin_amdgcn_raw_buffer_load_b64(rws, 0, 0, AUX_SC1);
    const v4u e4 = {EMPTY, EMPTY, EMPTY, EMPTY};
    __builtin_amdgcn_raw_buffer_store_b128(e4, rws, WS_O + lane * 16, j * O_STRIDE, AUX_SC1);
    if (lane == 0) __builtin_amdgcn_raw_buffer_store_b128(e4, rws, WS_O + 1024, j * O_STRIDE, AUX_SC1);
#pragma unroll
    for (int i = 0; i < 3; ++i) {
      const int q = i * 48 + lane;
      if (lane < 48) __builtin_amdgcn_raw_buffer_store_b128(e4, rws, WS_F + (q >> 1) * 8192 + (q & 1) * 16, j * 32, AUX_SC1);
    }
    VM_DRAIN();
    const unsigned long long e1 = (((unsigned long long)ep.y << 32) | ep.x) + 1ull;
    if (lane == 0) __builtin_amdgcn_raw_buffer_store_b64(v2u{(unsigned)e1, (unsigned)(e1 >> 32)}, rws, WS_CF, j * 8, AUX_SC1);
    STAMP(1);
    lds_wait(FL + FL_FW, 1u);
    const int hh = lane >> 5;
    const v4f fw = *(LAS v4f*)(L + L_FW + hh * 4);
    const int oaddr = WS_O + (hh * 128 + (lane & 15) * 8 + ((lane >> 4) & 1) * 4) * 4;
    STAMP(2);
    lds_spin(FL + FL_VDONE, 4u);
    STAMP(3);
    v4f o = *(LAS v4f*)(L + L_ORED + lane * 4) * fw[0];
#pragma unroll
    for (int w = 1; w < 4; ++w) o += *(LAS v4f*)(L + L_ORED + w * 256 + lane * 4) * fw[w];
    __builtin_amdgcn_raw_buffer_store_b128(box(o), rws, oaddr, j * O_STRIDE, AUX_SC1);
    FL[FL_OPUB] = 1u;
    STAMP(4);
  } else {
    if (wave < W_F) {
      const int pw = wave == W_P0 ? 0 : wave - W_P1 + 1;
      const int r8 = lane >> 3, c8 = lane & 7;
      const int lcA = (j * 9) >> 5, rowA = 18 * ((j * 9) & 31), nA = min(162, D - rowA);
      const int stride = pw == 0 ? 8192 : 9216;
      const int row0 = pw == 0 ? (s >> 2) * 144 : (pw == 1 ? rowA : 0);
      const int n = pw == 0 ? 144 : (pw == 1 ? nA : 162 - nA);
      const int colb = pw == 0 ? h * 512 + (s & 3) * 128 : (lcA + pw - 1) * 128;
      if (n > 0) {
        const auto rw = RS((const char*)(pw == 0 ? wq : w1) + row0 * stride + colb, (n - 1) * stride + 128);
        const auto rx = RS(x, D * 2);
        unsigned xr[5];
#pragma unroll
        for (int i = 0; i < 5; ++i) xr[i] = __builtin_amdgcn_raw_buffer_load_b32(rx, lane * 4 + i * 256, 0, 0);
        const v2u ep = __builtin_amdgcn_raw_buffer_load_b64(rws, 0, 0, AUX_SC1);
        v4u w[21];
        int vo = r8 * stride + c8 * 16;
#pragma unroll
        for (int i = 0; i < 21; ++i) { w[i] = __builtin_amdgcn_raw_buffer_load_b128(rw, vo, 0, AUX_NT); vo += 8 * stride; }
        const unsigned short bias = __builtin_amdgcn_raw_buffer_load_b16(RS(b1, F * 2), lane < 18 ? lane * 2 + j * 36 : OOB, 0, 0);
        SB();
        STAMP(1);
        const int gout = pw == 0 ? WS_Q + j * 512 : (pw == 1 ? WS_H + (lcA * 5 + j - (32 * lcA) / 9) * 512 : WS_H + (lcA + 1) * 5 * 512);
        v4u msk;
#pragma unroll
        for (int v = 0; v < 4; ++v) msk[v] = ((c8 >> 1) == v) ? ((lane & 1) ? 0xFFFF0000u : 0x0000FFFFu) : 0u;
        float xv[10];
#pragma unroll
        for (int i = 0; i < 5; ++i) { xv[2 * i] = asf(xr[i] << 16); xv[2 * i + 1] = asf(xr[i] & 0xFFFF0000u); }
#pragma nounroll
        for (int rep = pw == 0 ? 1 : 0; rep < 2; ++rep) layernorm(xv, lane);
        LAS unsigned* XS = L + L_XS + pw * 768;
#pragma unroll
        for (int i = 0; i < 5; ++i)
          *(LAS v2u*)(XS + i * 128 + lane * 2) = v2u{asu(xv[2 * i]) | (asu(xv[2 * i]) >> 16), asu(xv[2 * i + 1]) | (asu(xv[2 * i + 1]) >> 16)};
        *(LAS v2u*)(XS + 640 + lane * 2) = v2u{0u, 0u};
        LDS_DRAIN();
        v4f acc = {0.f, 0.f, 0.f, 0.f};
#pragma unroll
        for (int i = 0; i < 21; ++i) acc = mfma(msk & XS[row0 + i * 8 + r8], w[i], acc);
        acc = fold(acc);
        const unsigned long long e1 = (((unsigned long long)ep.y << 32) | ep.x) + 1ull;
        const unsigned thi = (unsigned)e1, tlo = (unsigned)(e1 >> 32) & 0xFFu;
        if ((lane & 40) == 0) {
#pragma unroll
          for (int i = 0; i < 4; ++i)
            __builtin_amdgcn_raw_buffer_store_b64(v2u{(asu(acc[i]) & 0xFFFFFF00u) | tlo, thi}, rws, (c8 * 8 + (lane >> 4) * 4 + i) * 8, gout, AUX_SC1);
        }
        STAMP(2);
        __builtin_amdgcn_s_sleep(QDELAY);
        if (pw == 0) {
          v2u g[16];
#pragma nounroll
          for (int sp = 0; sp < SPIN_G; ++sp) {
            CBAR();
            bool ok = true;
#pragma unroll
            for (int i = 0; i < 16; ++i) g[i] = __builtin_amdgcn_raw_buffer_load_b64(rws, lane * 8 + (i & 7) * 512, WS_Q + h * 8192 + (i >> 3) * 4096, AUX_SC1);
#pragma unroll
            for (int i = 0; i < 16; ++i) ok &= (g[i].y == thi) & ((g[i].x & 0xFFu) == tlo);
            if (__all(ok)) break;
          }
#pragma unroll
          for (int cg = 0; cg < 4; ++cg) {
            const float q = (asf(g[cg].x & 0xFFFFFF00u) + asf(g[4 + cg].x & 0xFFFFFF00u)) + (asf(g[8 + cg].x & 0xFFFFFF00u) + asf(g[12 + cg].x & 0xFFFFFF00u));
            ((LAS unsigned short*)(L + L_QS))[cg * 64 + lane] = (unsigned short)cvtpk(q, q);
          }
          LDS_DRAIN();
          FL[FL_Q] = 1u;
        } else if (pw == 1) {
          const int f = j * 18 + lane, lc = f >> 6;
          const int cnt = lane < 18 ? (576 * lc + 575) / 162 - (32 * lc) / 9 + 1 : 0;
          v2u g[5];
#pragma nounroll
          for (int sp = 0; sp < SPIN_G; ++sp) {
            CBAR();
            bool ok = true;
#pragma unroll
            for (int k = 0; k < 5; ++k) g[k] = __builtin_amdgcn_raw_buffer_load_b64(rws, k < cnt ? WS_H + (lc * 5 + k) * 512 + (f & 63) * 8 : OOB, 0, AUX_SC1);
#pragma unroll
            for (int k = 0; k < 5; ++k) ok &= (k >= cnt) | ((g[k].y == thi) & ((g[k].x & 0xFFu) == tlo));
            if (__all(ok)) break;
          }
          float hsum = 0.f;
#pragma unroll
          for (int k = 0; k < 5; ++k) hsum += asf(g[k].x & 0xFFFFFF00u);
          const float t = rbf(rbf(hsum) + asf((unsigned)bias << 16));
          const float z2 = 2.0f * 0.7978845608028654f * (t + 0.044715f * t * t * t);
          const float ge = t / (1.0f + __builtin_amdgcn_exp2f(-z2 * LOG2E));
          if (lane < 32) L[L_HS + lane] = lane < 18 ? cvtpk(ge, ge) : 0u;
          LDS_DRAIN();
          FL[FL_H] = 1u;
        }
        STAMP(3);
      }
    }
    if (wave >= W_F || wave == W_P2) {
      const int fs = wave == W_P2 ? 4 : wave - W_F, g4 = lane >> 4, c = lane & 15;
      const auto ro = RS((const char*)wo + j * 18432, 18432), r2 = RS((const char*)w2 + j * 20736, 20736);
      v4u msk;
#pragma unroll
      for (int v = 0; v < 4; ++v) msk[v] = ((c >> 1) == v) ? ((lane & 1) ? 0xFFFF0000u : 0x0000FFFFu) : 0u;
      const bool live = fs < 4 || c < 8;
      const int vo = live ? g4 * 4608 + fs * 256 + c * 16 : OOB, v2 = live ? g4 * 1152 + fs * 256 + c * 16 : OOB - 5 * 4608;
      lds_wait(FL + FL_VISS, 4u);
      v4u wo_[4], w2_[5];
#pragma unroll
      for (int rg = 0; rg < 4; ++rg) wo_[rg] = __builtin_amdgcn_raw_buffer_load_b128(ro, vo, rg * 1152, AUX_NT);
#pragma unroll
      for (int rg = 0; rg < 5; ++rg) w2_[rg] = __builtin_amdgcn_raw_buffer_load_b128(r2, v2 + rg * 4608, 0, AUX_NT);
      SB();
      STAMP(4);
      lds_wait(FL + FL_H, 1u);
      unsigned hv[5];
#pragma unroll
      for (int rg = 0; rg < 5; ++rg) hv[rg] = L[L_HS + rg * 4 + g4];
      lds_wait(FL + FL_G, 1u);
      const float gw = asf(L[L_G + c]);
      lds_wait(FL + FL_OPUB, 1u);
      STAMP(5);
      v4u ov;
#pragma nounroll
      for (int sp = 0; sp < SPIN_G; ++sp) {
        CBAR();
        ov = __builtin_amdgcn_raw_buffer_load_b128(rws, WS_O + c * O_STRIDE + g4 * 16, h * 16 * O_STRIDE + s * 64, AUX_SC1);
        if (__all(full(ov))) break;
      }
      STAMP(6);
      v4f acc = {0.f, 0.f, 0.f, 0.f}, acc2 = {0.f, 0.f, 0.f, 0.f};
#pragma unroll
      for (int rg = 0; rg < 5; ++rg) acc2 = mfma(msk & hv[rg], w2_[rg], acc2);
#pragma unroll
      for (int rg = 0; rg < 4; ++rg) {
        const float u = row_sum(asf(ov[rg]) * gw);
        acc = mfma(msk & cvtpk(u, u), wo_[rg], acc);
      }
      acc += acc2;
      if (lane < 32 && live) __builtin_amdgcn_raw_buffer_store_b128(box(acc), rws, WS_F + (fs * 16 + c) * 8192 + g4 * 16, j * 32, AUX_SC1);
      lds_inc(L + FL_FPUB, lane);
      STAMP(7);
    }
  }
  if (wave == 15) { for (int sp = 0; sp < 100000 && __builtin_amdgcn_s_memrealtime() < t_begin + 4000ull; ++sp) __builtin_amdgcn_s_sleep(2); }
}

extern "C" void kernel_launch(void* const* d_in, const int* in_sizes, int n_in, void* d_out, int out_size, void* d_ws, size_t ws_size,
                              hipStream_t stream) {
  fused_decode<<<256, 1024, 0, stream>>>(d_in[0], d_in[1], d_in[2], d_in[3], d_in[4], d_in[6], d_in[7], d_in[8], d_in[9], d_out, d_ws);
}
```

```cpp
#include <hip/hip_runtime.h>
#include <stdint.h>

typedef unsigned v4u __attribute__((ext_vector_type(4)));
typedef unsigned v2u __attribute__((ext_vector_type(2)));
typedef float v4f __attribute__((ext_vector_type(4)));
typedef __bf16 bf16x8 __attribute__((ext_vector_type(8)));
typedef __bf16 bf16x2 __attribute__((ext_vector_type(2)));
#define LAS __attribute__((address_space(3)))

constexpr int D = 576, SK = 16384, WIN = 4096, F = 4608;
constexpr int KV_BYTES = 16 * SK * 128 * 2;
constexpr int WQ_BYTES = D * 4096 * 2, W1_BYTES = D * F * 2;
constexpr int R = 72;
constexpr int WS_CF = 1024  , WS_Q = 4096, WS_H = WS_Q + 256 * 512, WS_O = WS_H + 72 * 5 * 512, O_STRIDE = 1152,
              WS_F = WS_O + 256 * O_STRIDE, WS_END = WS_F + 256 * 2304;
constexpr unsigned EMPTY = 0xFFFFFFFFu;
constexpr int OOB = 0x7FFFFFFF;
#define AUX_STREAM 2
#define WIN_KV 7
#define HANDOVER 3
#define QDELAY 20
constexpr int AUX_NT = AUX_STREAM, AUX_SC1 = 16;
constexpr int SPIN_G = 20000, SPIN_L = 400000;
constexpr float LOG2E = 1.4426950408889634f;

constexpr int L_FLAG = 0;
constexpr int FL_Q = 0, FL_H = 1, FL_KISS = 2  , FL_VISS = 6, FL_P = 7  , FL_KDONE = 11, FL_FW = 12, FL_G = 13, FL_VDONE = 14,
              FL_OPUB = 15, FL_FPUB = 16;
constexpr int L_XS = 32  , L_QS = L_XS + 3 * 768  , L_HS = L_QS + 128  , L_ML = L_HS + 32  ,
              L_FW = L_ML + 16  , L_G = L_FW + 16  , L_ORED = L_G + 16  , L_P = L_ORED + 1024  ,
              L_END = L_P + 4096;
#define LDS_PIN (24 * 1024)

#define RS(p, n) __builtin_amdgcn_make_buffer_rsrc((void*)(p), 0, (int)(n), 0x00020000)
#define SB() __builtin_amdgcn_sched_barrier(0)
#define CBAR() asm volatile("" ::: "memory")
#define LDS_DRAIN() asm volatile("s_waitcnt lgkmcnt(0)" ::: "memory")
#define VM_DRAIN() asm volatile("s_waitcnt vmcnt(0)" ::: "memory")

#define STAMP(i) do {} while (0)

__device__ __forceinline__ float asf(unsigned u) { return __builtin_bit_cast(float, u); }
__device__ __forceinline__ unsigned asu(float f) { return __builtin_bit_cast(unsigned, f); }
__device__ __forceinline__ unsigned cvtpk(float lo, float hi) {
  bf16x2 r = {(__bf16)lo, (__bf16)hi};
  return __builtin_bit_cast(unsigned, r);
}
__device__ __forceinline__ float rbf(float f) { return asf(cvtpk(f, f) & 0xFFFF0000u); }
template <int CTRL> __device__ __forceinline__ float dpp(float x) {
  return asf(__builtin_amdgcn_mov_dpp(asu(x), CTRL, 0xf, 0xf, true));
}
constexpr int XOR1 = 0xB1, XOR2 = 0x4E, XOR7 = 0x141, ROR8 = 0x128, ROR4 = 0x124;
__device__ __forceinline__ float xrow_sum(float x) {
  auto s = __builtin_amdgcn_permlane16_swap(asu(x), asu(x), false, false);
  x = asf(s[0]) + asf(s[1]);
  auto t = __builtin_amdgcn_permlane32_swap(asu(x), asu(x), false, false);
  return asf(t[0]) + asf(t[1]);
}
__device__ __forceinline__ float xrow_max(float x) {
  auto s = __builtin_amdgcn_permlane16_swap(asu(x), asu(x), false, false);
  x = fmaxf(asf(s[0]), asf(s[1]));
  auto t = __builtin_amdgcn_permlane32_swap(asu(x), asu(x), false, false);
  return fmaxf(asf(t[0]), asf(t[1]));
}
__device__ __forceinline__ float wave_sum(float x) {
  x += dpp<XOR1>(x); x += dpp<XOR2>(x); x += dpp<XOR7>(x); x += dpp<ROR8>(x);
  return xrow_sum(x);
}
__device__ __forceinline__ v4f mfma(v4u a, v4u b, v4f c) {
  return __builtin_amdgcn_mfma_f32_16x16x32_bf16(__builtin_bit_cast(bf16x8, a), __builtin_bit_cast(bf16x8, b), c, 0, 0, 0);
}
__device__ __forceinline__ float dot2(unsigned a, unsigned b, float c) {
  return __builtin_amdgcn_fdot2_f32_bf16(__builtin_bit_cast(bf16x2, a), __builtin_bit_cast(bf16x2, b), c, false);
}
__device__ __forceinline__ v4f fold(v4f d) {
  v4f r;
#pragma unroll
  for (int i = 0; i < 4; ++i) {
    const unsigned z = asu(dpp<ROR8>(d[i]));
    auto s = __builtin_amdgcn_permlane32_swap(z, z, false, false);
    r[i] = d[i] + asf(s[1]);
  }
  return r;
}
__device__ __forceinline__ v4u box(v4f v) {
  v4u r;
#pragma unroll
  for (int i = 0; i < 4; ++i) r[i] = min(asu(v[i]), 0xFFFFFFFEu);
  return r;
}
__device__ __forceinline__ bool full(v4u v) { return (v.x != EMPTY) & (v.y != EMPTY) & (v.z != EMPTY) & (v.w != EMPTY); }
__device__ __forceinline__ void lds_wait(volatile LAS unsigned* f, unsigned need) {
#pragma nounroll
  for (int sp = 0; sp < SPIN_L; ++sp) {
    if ((unsigned)__builtin_amdgcn_readfirstlane(*f) >= need) break;
    __builtin_amdgcn_s_sleep(1);
  }
  CBAR();
}
__device__ __forceinline__ void layernorm(float (&v)[10], int lane) {
  float s = 0.f;
#pragma unroll
  for (int i = 0; i < 10; ++i) s += v[i];
  const float mean = wave_sum(s) * (1.0f / D);
  float s2 = 0.f, s3 = 0.f;
#pragma unroll
  for (int i = 0; i < 8; ++i) { const float d = v[i] - mean; s2 = fmaf(d, d, s2); }
#pragma unroll
  for (int i = 8; i < 10; ++i) { const float d = v[i] - mean; s3 = fmaf(d, d, s3); }
  s2 += lane < 32 ? s3 : 0.f;
  const float var = wave_sum(s2) * (1.0f / D);
  const float mb = rbf(mean), sd = rbf(__builtin_sqrtf(rbf(rbf(var) + 1e-5f)));
  const float inv = 1.0f / sd;
#pragma unroll
  for (int i = 0; i < 10; ++i) v[i] = rbf(rbf(v[i] - mb) * inv);
  if (lane >= 32) { v[8] = 0.f; v[9] = 0.f; }
}

template <int N> __device__ __forceinline__ void vmwait() { asm volatile("s_waitcnt vmcnt(%0)" ::"i"(N) : "memory"); }
__device__ __forceinline__ void lds_spin(volatile LAS unsigned* f, unsigned need) {
#pragma nounroll
  for (int sp = 0; sp < SPIN_L; ++sp)
    if ((unsigned)__builtin_amdgcn_readfirstlane(*f) >= need) break;
  CBAR();
}
__device__ __forceinline__ void lds_inc(LAS unsigned* f, int lane) {
  if (lane == 0) __hip_atomic_fetch_add(f, 1u, __ATOMIC_RELAXED, __HIP_MEMORY_SCOPE_WORKGROUP);
}
__device__ __forceinline__ float row_sum(float x) {
  x += dpp<XOR1>(x); x += dpp<XOR2>(x); x += dpp<XOR7>(x); x += dpp<ROR8>(x);
  return x;
}
__device__ __forceinline__ float row_max(float x) {
  x = fmaxf(x, dpp<XOR1>(x)); x = fmaxf(x, dpp<XOR2>(x)); x = fmaxf(x, dpp<XOR7>(x)); x = fmaxf(x, dpp<ROR8>(x));
  return x;
}

constexpr int W_P0 = 0, W_K = 1, W_P1 = 5, W_P2 = 6, W_C = 7, W_V = 8, W_F = 12;

__global__ void __launch_bounds__(1024, 4)
fused_decode(const void* __restrict__ x, const void* __restrict__ kc, const void* __restrict__ vc, const void* __restrict__ wq,
             const void* __restrict__ wo, const void* __restrict__ w1, const void* __restrict__ b1, const void* __restrict__ w2,
             const void* __restrict__ b2, void* __restrict__ out, void* __restrict__ ws) {
  __shared__ __attribute__((aligned(16))) unsigned smem[LDS_PIN];
  LAS unsigned* L = (LAS unsigned*)smem;
  volatile LAS unsigned* FL = (volatile LAS unsigned*)smem;
  const int tid = threadIdx.x, lane = tid & 63, wave = __builtin_amdgcn_readfirstlane(tid >> 6);
  const int b = blockIdx.x, j = (b & 7) * 32 + (b >> 3), h = j >> 4, s = j & 15;
  STAMP(0);
  const unsigned long long t_begin = __builtin_amdgcn_s_memrealtime();
  unsigned long long t_ev = 0;

  if (b == 0 && wave == 0) {
    const auto rp_ = RS(kc, 0x7FFFFFF0);
    unsigned a0 = __builtin_amdgcn_raw_buffer_load_b32(rp_, (12288 * 256), 0, 2);
    asm volatile("s_waitcnt vmcnt(0)" : "+v"(a0) :: "memory");
    const unsigned long long t1_ = __builtin_amdgcn_s_memrealtime();
    asm volatile("s_waitcnt lgkmcnt(0)" ::: "memory");
    unsigned a1 = __builtin_amdgcn_raw_buffer_load_b32(rp_, (12288 * 256) + 1024, 0, 2);
    asm volatile("s_waitcnt vmcnt(0)" : "+v"(a1) :: "memory");
    a1 = __builtin_amdgcn_raw_buffer_load_b32(rp_, (12288 * 256) + 2048, 0, 2);
    asm volatile("s_waitcnt vmcnt(0)" : "+v"(a1) :: "memory");
    const unsigned long long t2_ = __builtin_amdgcn_s_memrealtime();
    t_ev = 1 ? t1_ - t_begin : t2_ - t1_;
  } else { for (int sp = 0; sp < 100000 && __builtin_amdgcn_s_memrealtime() < t_begin + 1000ull; ++sp) __builtin_amdgcn_s_sleep(2); }
  __syncthreads();

  if (tid < 32) L[L_FLAG + tid] = 0u;
  __syncthreads();
  const auto rws = RS(ws, WS_END);
  const int kvbase = (h * SK + (SK - WIN) + s * 256) * 256, rot = s & 7;

  if (wave >= W_K && wave < W_K + 4) {
    const int aw = wave - W_K;
    const auto rk = RS(kc, KV_BYTES);
    v4u kk[16];
#pragma unroll
    for (int i = 0; i < 16; ++i) {
      if (i >= WIN_KV) vmwait<WIN_KV - 1>();
      kk[i] = __builtin_amdgcn_raw_buffer_load_b128(rk, lane * 16, kvbase + aw * 1024 + ((i + rot) & 15) * 4096, AUX_NT);
      SB();
    }
    vmwait<HANDOVER>();
    FL[FL_KISS + aw] = 1u;
    STAMP(1);
    const bool reducer = aw == 1 && (j & 31) < 9;
    const int red = (j >> 5) * 9 + (j & 31);
    unsigned long long e1 = 0;
    unsigned xb = 0, bb = 0;
    const int ocol = red * 8 + (lane & 1) * 4 + (lane >> 4);
    if (aw == 0 || reducer) {
      const v2u ep = __builtin_amdgcn_raw_buffer_load_b64(rws, 0, 0, AUX_SC1);
      e1 = (((unsigned long long)ep.y << 32) | ep.x) + 1ull;
      xb = __builtin_amdgcn_raw_buffer_load_b16(RS(x, D * 2), ocol * 2, 0, 0);
      bb = __builtin_amdgcn_raw_buffer_load_b16(RS(b2, D * 2), ocol * 2, 0, 0);
#pragma nounroll
      for (int sp = 0; sp < SPIN_G; ++sp) {
        CBAR();
        bool ok = true;
#pragma unroll
        for (int i = 0; i < 4; ++i) {
          const v2u cf = __builtin_amdgcn_raw_buffer_load_b64(rws, WS_CF + (aw == 0 ? h * 128 + (lane & 15) * 8 : lane * 8 + i * 512), 0, AUX_SC1);
          ok &= (cf.x == (unsigned)e1) & (cf.y == (unsigned)(e1 >> 32));
        }
        if (__all(ok)) break;
      }
    }
    lds_wait(FL + FL_Q, 1u);
    STAMP(2);
    const int c = lane & 15, hb = c >> 3;
    const v4u qa = *(LAS v4u*)(L + L_QS + hb * 64 + c * 4), qb = *(LAS v4u*)(L + L_QS + (1 - hb) * 64 + c * 4);
    float sc[16];
#pragma unroll
    for (int i = 0; i < 16; ++i) {
      float a = 0.f, bq = 0.f;
#pragma unroll
      for (int t = 0; t < 4; ++t) { a = dot2(kk[i][t], qa[t], a); bq = dot2(kk[i][t], qb[t], bq); }
      float u = a + dpp<ROR8>(bq);
      u += dpp<XOR1>(u); u += dpp<XOR2>(u); u += dpp<XOR7>(u);
      sc[i] = rbf(rbf(u) * 0.08838834764831845f);
    }
    float m = sc[0];
#pragma unroll
    for (int i = 1; i < 16; ++i) m = fmaxf(m, sc[i]);
    m = xrow_max(m);
    float l = 0.f;
#pragma unroll
    for (int i4 = 0; i4 < 4; ++i4) {
      v4u pk;
#pragma unroll
      for (int t = 0; t < 4; ++t) { const float p = __builtin_amdgcn_exp2f((sc[i4 * 4 + t] - m) * LOG2E); l += p; pk[t] = cvtpk(p, p); }
      *(LAS v4u*)(L + L_P + aw * 1024 + i4 * 256 + lane * 4) = pk;
    }
    l = xrow_sum(l);
    if ((lane & 55) == 0) *(LAS v2u*)(L + L_ML + aw * 4 + hb * 2) = v2u{asu(m), asu(l)};
    LDS_DRAIN();
    FL[FL_P + aw] = 1u;
    lds_inc(L + FL_KDONE, lane);
    STAMP(3);
    if (aw == 0) {
      lds_wait(FL + FL_KDONE, 4u);
      const int hh = lane >> 5, w = lane & 3;
      const v2u ml = *(LAS v2u*)(L + L_ML + w * 4 + hh * 2);
      float M = asf(ml.x);
      M = fmaxf(M, dpp<XOR1>(M)); M = fmaxf(M, dpp<XOR2>(M));
      const float fw = __builtin_amdgcn_exp2f((asf(ml.x) - M) * LOG2E);
      float Ls = asf(ml.y) * fw;
      Ls += dpp<XOR1>(Ls); Ls += dpp<XOR2>(Ls);
      if ((lane & 28) == 0) L[L_FW + hh * 4 + w] = asu(fw);
      if ((lane & 31) == 0)
        __builtin_amdgcn_raw_buffer_store_b64(v2u{min(asu(M), 0xFFFFFFFEu), min(asu(Ls), 0xFFFFFFFEu)}, rws, WS_O + 1024 + hh * 8, j * O_STRIDE, AUX_SC1);
      LDS_DRAIN();
      FL[FL_FW] = 1u;
      STAMP(4);
      v2u g;
#pragma nounroll
      for (int sp = 0; sp < SPIN_G; ++sp) {
        CBAR();
        g = __builtin_amdgcn_raw_buffer_load_b64(rws, WS_O + 1024 + (lane & 15) * O_STRIDE, h * 16 * O_STRIDE + (s >> 3) * 8, AUX_SC1);
        if (__all((g.x != EMPTY) & (g.y != EMPTY))) break;
      }
      const float Mg = row_max(asf(g.x));
      const float gw = __builtin_amdgcn_exp2f((asf(g.x) - Mg) * LOG2E);
      const float Lg = row_sum(asf(g.y) * gw);
      if (lane < 16) L[L_G + lane] = asu(gw / Lg);
      LDS_DRAIN();
      FL[FL_G] = 1u;
      STAMP(5);
    } else if (reducer) {
      lds_wait(FL + FL_FPUB, 5u);
      STAMP(4);
      v4f sum;
      bool done = false;
#pragma nounroll
      for (int sp = 0; sp < SPIN_G; ++sp) {
        CBAR();
        if (!done) {
          v4u pv[8];
#pragma unroll
          for (int i = 0; i < 8; ++i) pv[i] = __builtin_amdgcn_raw_buffer_load_b128(rws, lane * 16 + (i & 3) * 1024, WS_F + red * 8192 + (i >> 2) * 4096, AUX_SC1);
          unsigned mx = 0;
          sum = __builtin_bit_cast(v4f, pv[0]);
#pragma unroll
          for (int i = 1; i < 8; ++i) sum += __builtin_bit_cast(v4f, pv[i]);
          done = true;
          const float chk = (sum[0] + sum[1]) + (sum[2] + sum[3]);
          if (chk != chk) {
#pragma unroll
            for (int i = 0; i < 8; ++i) mx = max(max(mx, max(pv[i].x, pv[i].y)), max(pv[i].z, pv[i].w));
            done = mx != EMPTY;
          }
        }
        if (__all(done)) break;
      }
      STAMP(5);
      auto s02 = __builtin_amdgcn_permlane32_swap(asu(sum[0]), asu(sum[2]), false, false);
      auto s13 = __builtin_amdgcn_permlane32_swap(asu(sum[1]), asu(sum[3]), false, false);
      const float u0 = asf(s02[0]) + asf(s02[1]), u1 = asf(s13[0]) + asf(s13[1]);
      auto s01 = __builtin_amdgcn_permlane16_swap(asu(u0), asu(u1), false, false);
      float u = asf(s01[0]) + asf(s01[1]);
      u += dpp<ROR8>(u); u += dpp<ROR4>(u); u += dpp<XOR2>(u);
      u += asf(xb << 16) + asf(bb << 16);
      if ((lane & 14) == 0) __builtin_amdgcn_raw_buffer_store_b16((unsigned short)cvtpk(u, u), RS(out, D * 2), ocol * 2, 0, 0);
      if (j == 0 && lane == 0) __builtin_amdgcn_raw_buffer_store_b64(v2u{(unsigned)e1, (unsigned)(e1 >> 32)}, rws, 0, 0, AUX_SC1);
      STAMP(6);
    }
  } else if (wave >= W_V && wave < W_V + 4) {
    const int aw = wave - W_V;
    const auto rv = RS(vc, KV_BYTES);
    v4u msk;
#pragma unroll
    for (int v = 0; v < 4; ++v) msk[v] = (((lane & 7) >> 1) == v) ? ((lane & 1) ? 0xFFFF0000u : 0x0000FFFFu) : 0u;
    lds_wait(FL + FL_KISS + aw, 1u);
    STAMP(1);
    v4u vv[16];
#pragma unroll
    for (int i = 0; i < 16; ++i) {
      if (i >= WIN_KV) vmwait<WIN_KV - 1>();
      vv[i] = __builtin_amdgcn_raw_buffer_load_b128(rv, lane * 16, kvbase + aw * 1024 + ((i + rot) & 15) * 4096, AUX_NT);
      SB();
    }
    vmwait<HANDOVER>();
    lds_inc(L + FL_VISS, lane);
    STAMP(2);
    lds_wait(FL + FL_P + aw, 1u);
    STAMP(3);
    v4u pk[4];
#pragma unroll
    for (int i4 = 0; i4 < 4; ++i4) pk[i4] = *(LAS v4u*)(L + L_P + aw * 1024 + i4 * 256 + lane * 4);
    v4f acc = {0.f, 0.f, 0.f, 0.f};
#pragma unroll
    for (int i = 0; i < 16; ++i) acc = mfma(msk & pk[i >> 2][i & 3], vv[i], acc);
    *(LAS v4f*)(L + L_ORED + aw * 256 + lane * 4) = acc;
    LDS_DRAIN();
    lds_inc(L + FL_VDONE, lane);
    STAMP(4);
  } else if (wave == W_C) {
    const v2u ep = __builtin_amdgcn_raw_buffer_load_b64(rws, 0, 0, AUX_SC1);
    const v4u e4 = {EMPTY, EMPTY, EMPTY, EMPTY};
    __builtin_amdgcn_raw_buffer_store_b128(e4, rws, WS_O + lane * 16, j * O_STRIDE, AUX_SC1);
    if (lane == 0) __builtin_amdgcn_raw_buffer_store_b128(e4, rws, WS_O + 1024, j * O_STRIDE, AUX_SC1);
#pragma unroll
    for (int i = 0; i < 3; ++i) {
      const int q = i * 48 + lane;
      if (lane < 48) __builtin_amdgcn_raw_buffer_store_b128(e4, rws, WS_F + (q >> 1) * 8192 + (q & 1) * 16, j * 32, AUX_SC1);
    }
    VM_DRAIN();
    const unsigned long long e1 = (((unsigned long long)ep.y << 32) | ep.x) + 1ull;
    if (lane == 0) __builtin_amdgcn_raw_buffer_store_b64(v2u{(unsigned)e1, (unsigned)(e1 >> 32)}, rws, WS_CF, j * 8, AUX_SC1);
    STAMP(1);
    lds_wait(FL + FL_FW, 1u);
    const int hh = lane >> 5;
    const v4f fw = *(LAS v4f*)(L + L_FW + hh * 4);
    const int oaddr = WS_O + (hh * 128 + (lane & 15) * 8 + ((lane >> 4) & 1) * 4) * 4;
    STAMP(2);
    lds_spin(FL + FL_VDONE, 4u);
    STAMP(3);
    v4f o = *(LAS v4f*)(L + L_ORED + lane * 4) * fw[0];
#pragma unroll
    for (int w = 1; w < 4; ++w) o += *(LAS v4f*)(L + L_ORED + w * 256 + lane * 4) * fw[w];
    __builtin_amdgcn_raw_buffer_store_b128(box(o), rws, oaddr, j * O_STRIDE, AUX_SC1);
    FL[FL_OPUB] = 1u;
    STAMP(4);
  } else {
    if (wave < W_F) {
      const int pw = wave == W_P0 ? 0 : wave - W_P1 + 1;
      const int r8 = lane >> 3, c8 = lane & 7;
      const int lcA = (j * 9) >> 5, rowA = 18 * ((j * 9) & 31), nA = min(162, D - rowA);
      const int stride = pw == 0 ? 8192 : 9216;
      const int row0 = pw == 0 ? (s >> 2) * 144 : (pw == 1 ? rowA : 0);
      const int n = pw == 0 ? 144 : (pw == 1 ? nA : 162 - nA);
      const int colb = pw == 0 ? h * 512 + (s & 3) * 128 : (lcA + pw - 1) * 128;
      if (n > 0) {
        const auto rw = RS((const char*)(pw == 0 ? wq : w1) + row0 * stride + colb, (n - 1) * stride + 128);
        const auto rx = RS(x, D * 2);
        unsigned xr[5];
#pragma unroll
        for (int i = 0; i < 5; ++i) xr[i] = __builtin_amdgcn_raw_buffer_load_b32(rx, lane * 4 + i * 256, 0, 0);
        const v2u ep = __builtin_amdgcn_raw_buffer_load_b64(rws, 0, 0, AUX_SC1);
        v4u w[21];
        int vo = r8 * stride + c8 * 16;
#pragma unroll
        for (int i = 0; i < 21; ++i) { w[i] = __builtin_amdgcn_raw_buffer_load_b128(rw, vo, 0, AUX_NT); vo += 8 * stride; }
        const unsigned short bias = __builtin_amdgcn_raw_buffer_load_b16(RS(b1, F * 2), lane < 18 ? lane * 2 + j * 36 : OOB, 0, 0);
        SB();
        STAMP(1);
        const int gout = pw == 0 ? WS_Q + j * 512 : (pw == 1 ? WS_H + (lcA * 5 + j - (32 * lcA) / 9) * 512 : WS_H + (lcA + 1) * 5 * 512);
        v4u msk;
#pragma unroll
        for (int v = 0; v < 4; ++v) msk[v] = ((c8 >> 1) == v) ? ((lane & 1) ? 0xFFFF0000u : 0x0000FFFFu) : 0u;
        float xv[10];
#pragma unroll
        for (int i = 0; i < 5; ++i) { xv[2 * i] = asf(xr[i] << 16); xv[2 * i + 1] = asf(xr[i] & 0xFFFF0000u); }
#pragma nounroll
        for (int rep = pw == 0 ? 1 : 0; rep < 2; ++rep) layernorm(xv, lane);
        LAS unsigned* XS = L + L_XS + pw * 768;
#pragma unroll
        for (int i = 0; i < 5; ++i)
          *(LAS v2u*)(XS + i * 128 + lane * 2) = v2u{asu(xv[2 * i]) | (asu(xv[2 * i]) >> 16), asu(xv[2 * i + 1]) | (asu(xv[2 * i + 1]) >> 16)};
        *(LAS v2u*)(XS + 640 + lane * 2) = v2u{0u, 0u};
        LDS_DRAIN();
        v4f acc = {0.f, 0.f, 0.f, 0.f};
#pragma unroll
        for (int i = 0; i < 21; ++i) acc = mfma(msk & XS[row0 + i * 8 + r8], w[i], acc);
        acc = fold(acc);
        const unsigned long long e1 = (((unsigned long long)ep.y << 32) | ep.x) + 1ull;
        const unsigned thi = (unsigned)e1, tlo = (unsigned)(e1 >> 32) & 0xFFu;
        if ((lane & 40) == 0) {
#pragma unroll
          for (int i = 0; i < 4; ++i)
            __builtin_amdgcn_raw_buffer_store_b64(v2u{(asu(acc[i]) & 0xFFFFFF00u) | tlo, thi}, rws, (c8 * 8 + (lane >> 4) * 4 + i) * 8, gout, AUX_SC1);
        }
        STAMP(2);
        __builtin_amdgcn_s_sleep(QDELAY);
        if (pw == 0) {
          v2u g[16];
#pragma nounroll
          for (int sp = 0; sp < SPIN_G; ++sp) {
            CBAR();
            bool ok = true;
#pragma unroll
            for (int i = 0; i < 16; ++i) g[i] = __builtin_amdgcn_raw_buffer_load_b64(rws, lane * 8 + (i & 7) * 512, WS_Q + h * 8192 + (i >> 3) * 4096, AUX_SC1);
#pragma unroll
            for (int i = 0; i < 16; ++i) ok &= (g[i].y == thi) & ((g[i].x & 0xFFu) == tlo);
            if (__all(ok)) break;
          }
#pragma unroll
          for (int cg = 0; cg < 4; ++cg) {
            const float q = (asf(g[cg].x & 0xFFFFFF00u) + asf(g[4 + cg].x & 0xFFFFFF00u)) + (asf(g[8 + cg].x & 0xFFFFFF00u) + asf(g[12 + cg].x & 0xFFFFFF00u));
            ((LAS unsigned short*)(L + L_QS))[cg * 64 + lane] = (unsigned short)cvtpk(q, q);
          }
          LDS_DRAIN();
          FL[FL_Q] = 1u;
        } else if (pw == 1) {
          const int f = j * 18 + lane, lc = f >> 6;
          const int cnt = lane < 18 ? (576 * lc + 575) / 162 - (32 * lc) / 9 + 1 : 0;
          v2u g[5];
#pragma nounroll
          for (int sp = 0; sp < SPIN_G; ++sp) {
            CBAR();
            bool ok = true;
#pragma unroll
            for (int k = 0; k < 5; ++k) g[k] = __builtin_amdgcn_raw_buffer_load_b64(rws, k < cnt ? WS_H + (lc * 5 + k) * 512 + (f & 63) * 8 : OOB, 0, AUX_SC1);
#pragma unroll
            for (int k = 0; k < 5; ++k) ok &= (k >= cnt) | ((g[k].y == thi) & ((g[k].x & 0xFFu) == tlo));
            if (__all(ok)) break;
          }
          float hsum = 0.f;
#pragma unroll
          for (int k = 0; k < 5; ++k) hsum += asf(g[k].x & 0xFFFFFF00u);
          const float t = rbf(rbf(hsum) + asf((unsigned)bias << 16));
          const float z2 = 2.0f * 0.7978845608028654f * (t + 0.044715f * t * t * t);
          const float ge = t / (1.0f + __builtin_amdgcn_exp2f(-z2 * LOG2E));
          if (lane < 32) L[L_HS + lane] = lane < 18 ? cvtpk(ge, ge) : 0u;
          LDS_DRAIN();
          FL[FL_H] = 1u;
        }
        STAMP(3);
      }
    }
    if (wave >= W_F || wave == W_P2) {
      const int fs = wave == W_P2 ? 4 : wave - W_F, g4 = lane >> 4, c = lane & 15;
      const auto ro = RS((const char*)wo + j * 18432, 18432), r2 = RS((const char*)w2 + j * 20736, 20736);
      v4u msk;
#pragma unroll
      for (int v = 0; v < 4; ++v) msk[v] = ((c >> 1) == v) ? ((lane & 1) ? 0xFFFF0000u : 0x0000FFFFu) : 0u;
      const bool live = fs < 4 || c < 8;
      const int vo = live ? g4 * 4608 + fs * 256 + c * 16 : OOB, v2 = live ? g4 * 1152 + fs * 256 + c * 16 : OOB - 5 * 4608;
      lds_wait(FL + FL_VISS, 4u);
      v4u wo_[4], w2_[5];
#pragma unroll
      for (int rg = 0; rg < 4; ++rg) wo_[rg] = __builtin_amdgcn_raw_buffer_load_b128(ro, vo, rg * 1152, AUX_NT);
#pragma unroll
      for (int rg = 0; rg < 5; ++rg) w2_[rg] = __builtin_amdgcn_raw_buffer_load_b128(r2, v2 + rg * 4608, 0, AUX_NT);
      SB();
      STAMP(4);
      lds_wait(FL + FL_H, 1u);
      unsigned hv[5];
#pragma unroll
      for (int rg = 0; rg < 5; ++rg) hv[rg] = L[L_HS + rg * 4 + g4];
      lds_wait(FL + FL_G, 1u);
      const float gw = asf(L[L_G + c]);
      lds_wait(FL + FL_OPUB, 1u);
      STAMP(5);
      v4u ov;
#pragma nounroll
      for (int sp = 0; sp < SPIN_G; ++sp) {
        CBAR();
        ov = __builtin_amdgcn_raw_buffer_load_b128(rws, WS_O + c * O_STRIDE + g4 * 16, h * 16 * O_STRIDE + s * 64, AUX_SC1);
        if (__all(full(ov))) break;
      }
      STAMP(6);
      v4f acc = {0.f, 0.f, 0.f, 0.f}, acc2 = {0.f, 0.f, 0.f, 0.f};
#pragma unroll
      for (int rg = 0; rg < 5; ++rg) acc2 = mfma(msk & hv[rg], w2_[rg], acc2);
#pragma unroll
      for (int rg = 0; rg < 4; ++rg) {
        const float u = row_sum(asf(ov[rg]) * gw);
        acc = mfma(msk & cvtpk(u, u), wo_[rg], acc);
      }
      acc += acc2;
      if (lane < 32 && live) __builtin_amdgcn_raw_buffer_store_b128(box(acc), rws, WS_F + (fs * 16 + c) * 8192 + g4 * 16, j * 32, AUX_SC1);
      lds_inc(L + FL_FPUB, lane);
      STAMP(7);
    }
  }
  if (b == 0 && wave == 0) { for (int sp = 0; sp < 100000 && __builtin_amdgcn_s_memrealtime() < t_begin + 6000ull + 10ull * t_ev; ++sp) __builtin_amdgcn_s_sleep(2); }
}

extern "C" void kernel_launch(void* const* d_in, const int* in_sizes, int n_in, void* d_out, int out_size, void* d_ws, size_t ws_size,
                              hipStream_t stream) {
  fused_decode<<<256, 1024, 0, stream>>>(d_in[0], d_in[1], d_in[2], d_in[3], d_in[4], d_in[6], d_in[7], d_in[8], d_in[9], d_out, d_ws);
}
```

```cpp
#include <hip/hip_runtime.h>
#include <stdint.h>
#include <type_traits>

typedef unsigned v4u __attribute__((ext_vector_type(4)));
typedef unsigned v2u __attribute__((ext_vector_type(2)));
typedef float v4f __attribute__((ext_vector_type(4)));
typedef float v2f __attribute__((ext_vector_type(2)));
typedef short v4s __attribute__((ext_vector_type(4)));
typedef __bf16 bf16x8 __attribute__((ext_vector_type(8)));
typedef __bf16 bf16x2 __attribute__((ext_vector_type(2)));
#define LAS __attribute__((address_space(3)))

constexpr int D = 576, SK = 16384, WIN = 4096, F = 4608;
constexpr int KV_BYTES = 16 * SK * 128 * 2;
constexpr int WQ_BYTES = D * 4096 * 2, W1_BYTES = D * F * 2;
constexpr int WS_GC = 1024  , WS_Q = 4096, WS_H = WS_Q + 256 * 512, WS_O = WS_H + 72 * 5 * 512, O_STRIDE = 1152,
              WS_F = WS_O + 256 * O_STRIDE, WS_END = WS_F + 256 * 2304;
constexpr unsigned EMPTY = 0xFFFFFFFFu;
constexpr int OOB = 0x7FFFFFFF;
#define WINDOW 11
#define PACE10 154
#define PACE0 50
#define LAG 4
#define LAG1 5
#define LAG2 9
#define LAG3 5
#define TQ 385
#define TH 520
#define TM (slot_time(42) + 95)
#define SETTLE 60
#define TO 60
#define FDELAY 8
#define CODE_BYTES 13248
constexpr int AUX_NT = 2, AUX_SC1 = 16, W = WINDOW;
#define ABL 0
constexpr int SPIN_G = ABL ? 1 : 20000, SPIN_L = 400000;
constexpr float LOG2E = 1.4426950408889634f;

constexpr int L_WQ = 0  , L_K = 5120  , L_W1 = L_K + 16384  , L_FLAG = L_W1 + 6144  ;
constexpr int FL_Q = 0, FL_H = 1, FL_PROG = 4  , FL_P = 8  , FL_KDONE = 13, FL_FW = 14, FL_G = 15, FL_VDONE = 16, FL_OPUB = 17,
              FL_FPUB = 18;
constexpr int L_XR = L_FLAG + 32  , L_Y = L_XR + 2 * 768  , L_QS = L_Y + 2 * 512  , L_HS = L_QS + 128  ,
              L_ML = L_HS + 32  , L_FW = L_ML + 16  , L_G = L_FW + 16  , L_ORED = L_G + 16  ,
              L_P = L_ORED + 1280  , L_END = L_P + 512;
constexpr int YZ = 192;

#define RS(p, n) __builtin_amdgcn_make_buffer_rsrc((void*)(p), 0, (int)(n), 0x00020000)
#define SB() __builtin_amdgcn_sched_barrier(0)
#define CBAR() asm volatile("" ::: "memory")
#define LDS_DRAIN() asm volatile("s_waitcnt lgkmcnt(0)" ::: "memory")
#define VM_DRAIN() asm volatile("s_waitcnt vmcnt(0)" ::: "memory")
#define VM_CLEAR() __builtin_amdgcn_s_waitcnt(0x0F70)
#define PIN(x) asm volatile("" : "+v"(x))

#define COUNT() do {} while (0)
#define STAMP(i) do {} while (0)

__device__ __forceinline__ float asf(unsigned u) { return __builtin_bit_cast(float, u); }
__device__ __forceinline__ unsigned asu(float f) { return __builtin_bit_cast(unsigned, f); }
__device__ __forceinline__ unsigned cvtpk(float lo, float hi) {
  return __builtin_bit_cast(unsigned, __builtin_convertvector(v2f{lo, hi}, bf16x2));
}
__device__ __forceinline__ v2f unpk(unsigned p) { return v2f{asf(p << 16), asf(p & 0xFFFF0000u)}; }
__device__ __forceinline__ float rbf(float f) { return asf(cvtpk(0.f, f)); }
__device__ __forceinline__ float vmax(float a, float b) { float r; asm("v_max_f32 %0, %1, %2" : "=v"(r) : "v"(a), "v"(b)); return r; }
__device__ __forceinline__ float vmax3(float a, float b, float c) { float r; asm("v_max3_f32 %0, %1, %2, %3" : "=v"(r) : "v"(a), "v"(b), "v"(c)); return r; }
template <int CTRL> __device__ __forceinline__ float dpp(float x) {
  return asf(__builtin_amdgcn_mov_dpp(asu(x), CTRL, 0xf, 0xf, true));
}
constexpr int XOR1 = 0xB1, XOR2 = 0x4E, XOR7 = 0x141, ROR8 = 0x128, ROR4 = 0x124;
__device__ __forceinline__ float xrow_sum(float x) {
  auto s = __builtin_amdgcn_permlane16_swap(asu(x), asu(x), false, false);
  x = asf(s[0]) + asf(s[1]);
  auto t = __builtin_amdgcn_permlane32_swap(asu(x), asu(x), false, false);
  return asf(t[0]) + asf(t[1]);
}
__device__ __forceinline__ float xrow_max(float x) {
  auto s = __builtin_amdgcn_permlane16_swap(asu(x), asu(x), false, false);
  x = fmaxf(asf(s[0]), asf(s[1]));
  auto t = __builtin_amdgcn_permlane32_swap(asu(x), asu(x), false, false);
  return fmaxf(asf(t[0]), asf(t[1]));
}
__device__ __forceinline__ float row_sum(float x) {
  x += dpp<XOR1>(x); x += dpp<XOR2>(x); x += dpp<XOR7>(x); x += dpp<ROR8>(x);
  return x;
}
__device__ __forceinline__ float row_max(float x) {
  x = fmaxf(x, dpp<XOR1>(x)); x = fmaxf(x, dpp<XOR2>(x)); x = fmaxf(x, dpp<XOR7>(x)); x = fmaxf(x, dpp<ROR8>(x));
  return x;
}
__device__ __forceinline__ v4f mfma(v4u a, v4u b, v4f c) {
  return __builtin_amdgcn_mfma_f32_16x16x32_bf16(__builtin_bit_cast(bf16x8, a), __builtin_bit_cast(bf16x8, b), c, 0, 0, 0);
}
__device__ __forceinline__ v4u box(v4f v) {
  v4u r;
#pragma unroll
  for (int i = 0; i < 4; ++i) r[i] = min(asu(v[i]), 0xFFFFFFFEu);
  return r;
}
__device__ __forceinline__ bool full(v4u v) { return max(max(v.x, v.y), max(v.z, v.w)) != EMPTY; }
template <int NAP = 2> __device__ __forceinline__ void lds_wait(volatile LAS unsigned* f, unsigned need) {
#pragma nounroll
  for (int sp = 0; sp < SPIN_L; ++sp) {
    if ((unsigned)__builtin_amdgcn_readfirstlane(*f) >= need) break;
    if (NAP) __builtin_amdgcn_s_sleep(NAP);
  }
  CBAR();
}
template <int NAP> __device__ __forceinline__ void lds_wait4(volatile LAS unsigned* f, unsigned need) {
#pragma nounroll
  for (int sp = 0; sp < SPIN_L; ++sp) {
    const v4u v = *(volatile LAS v4u*)f;
    if ((unsigned)__builtin_amdgcn_readfirstlane(min(min(v.x, v.y), min(v.z, v.w))) >= need) break;
    if (NAP) __builtin_amdgcn_s_sleep(NAP);
  }
  CBAR();
}
__device__ __forceinline__ void lds_inc(LAS unsigned* f, int lane) {
  if (lane == 0) __hip_atomic_fetch_add(f, 1u, __ATOMIC_RELAXED, __HIP_MEMORY_SCOPE_WORKGROUP);
}
__device__ __forceinline__ unsigned now() {
  unsigned long long t;
  asm volatile("s_memrealtime %0\n\ts_waitcnt lgkmcnt(0)" : "=s"(t)::"memory");
  return (unsigned)t;
}
template <int T> __device__ __forceinline__ void pace(unsigned t0) {
  asm volatile("1:\n\ts_memrealtime s[90:91]\n\ts_waitcnt lgkmcnt(0)\n\ts_sub_u32 s90, s90, %0\n\ts_cmpk_lt_u32 s90, %1\n\ts_cbranch_scc1 1b"
               ::"s"(t0), "i"(T) : "memory", "scc", "s90", "s91");
}
constexpr int slot_time(int n) { return PACE0 + (n - 3) * PACE10 / 10; }
template <int N> __device__ __forceinline__ void vmwait() { asm volatile("s_waitcnt vmcnt(%0)" ::"i"(N) : "memory"); }
__device__ __forceinline__ void st128(v4u d, __amdgpu_buffer_rsrc_t r, int voff, int soff) {
  __builtin_amdgcn_raw_buffer_store_b128(d, r, voff, soff, AUX_SC1);
  asm volatile("s_nop 1" ::"v"(d) : "memory");
}
__device__ __forceinline__ void dma(__amdgpu_buffer_rsrc_t r, int voff, int soff, int ldsaddr) {
  asm volatile("s_mov_b32 m0, %0\n\ts_nop 0\n\tbuffer_load_dwordx4 %1, %2, %3 offen nt lds" ::"s"(ldsaddr), "v"(voff), "s"(r), "s"(soff) : "memory");
}
__device__ __forceinline__ void dma_dev(__amdgpu_buffer_rsrc_t r, int voff, int ldsaddr) {
  asm volatile("s_mov_b32 m0, %0\n\ts_nop 0\n\tbuffer_load_dwordx4 %1, %2, 0 offen sc1 lds" ::"s"(ldsaddr), "v"(voff), "s"(r) : "memory");
}
__device__ __forceinline__ v4u keep_mask(int m) {
  v4u k;
#pragma unroll
  for (int v = 0; v < 4; ++v) k[v] = ((m >> 1) == v) ? ((m & 1) ? 0xFFFF0000u : 0x0000FFFFu) : 0u;
  return k;
}
template <int I, int N, class Fn> __device__ __forceinline__ void static_for(Fn&& fn) {
  if constexpr (I < N) { fn(std::integral_constant<int, I>{}); static_for<I + 1, N>(fn); }
}
__device__ __forceinline__ void layernorm(unsigned (&p)[5], int lane) {
  const float c0 = asf(__builtin_amdgcn_readfirstlane(p[0]) << 16);
  v2f v[5], d[5];
#pragma unroll
  for (int i = 0; i < 5; ++i) { v[i] = unpk(p[i]); d[i] = v[i] - c0; }
  if (lane >= 32) d[4] = v2f{0.f, 0.f};
  const v2f s1v = (d[0] + d[1]) + (d[2] + d[3]) + d[4];
  v2f s2v = d[0] * d[0];
#pragma unroll
  for (int i = 1; i < 5; ++i) s2v = __builtin_elementwise_fma(d[i], d[i], s2v);
  const float s1 = xrow_sum(row_sum(s1v.x + s1v.y)) * (1.0f / D), s2 = xrow_sum(row_sum(s2v.x + s2v.y)) * (1.0f / D);
  const float var = fmaxf(fmaf(-s1, s1, s2), 0.f);
  const float mb = rbf(c0 + s1), sd = rbf(__builtin_amdgcn_sqrtf(rbf(rbf(var) + 1e-5f)));
  const float inv = __builtin_amdgcn_rcpf(sd);
#pragma unroll
  for (int i = 0; i < 5; ++i) {
    const v2f t = v[i] - mb;
    const v2f u = v2f{rbf(t.x), rbf(t.y)} * inv;
    p[i] = cvtpk(u.x, u.y);
  }
}

constexpr int W_F4 = 4, W_C = 5, W_P = 6, W_A = 8, NWAVE = 12;

extern "C" __global__ void __launch_bounds__(NWAVE * 64, 3)
fused_decode(const void* __restrict__ wq, const void* __restrict__ kc, const void* __restrict__ w1, const void* __restrict__ vc,
             const void* __restrict__ wo, const void* __restrict__ w2, void* __restrict__ ws, const void* __restrict__ x,
             const void* __restrict__ b1, const void* __restrict__ b2, void* __restrict__ out) {
  __shared__ __attribute__((aligned(16))) unsigned smem[L_END];
  LAS unsigned* L = (LAS unsigned*)smem;
  volatile LAS unsigned* FL = (volatile LAS unsigned*)smem + L_FLAG;
  const int tid = threadIdx.x, lane = tid & 63, wave = __builtin_amdgcn_readfirstlane(tid >> 6);
  const int b = blockIdx.x, j = (b & 7) * 32 + (b >> 3), h = j >> 4, s = j & 15;
  const int r8 = lane >> 3, c8 = lane & 7, g4 = lane >> 4, c = lane & 15;
  const int lcA = (j * 9) >> 5, rowA = 18 * ((j * 9) & 31);
  const int lds0 = (int)(unsigned)(uintptr_t)L;
  const unsigned long long t00 = __builtin_amdgcn_s_memrealtime();
  const unsigned t0 = (unsigned)t00;
  if ((wave >> 1) == (W_P >> 1)) {
    const int dst = lds0 + (L_XR + (wave & 1) * 768) * 4;
    dma_dev(RS(x, D * 2), lane * 16, dst);
    dma_dev(RS(x, D * 2), lane * 16 + 1024, dst + 1024);
    dma_dev(RS(ws, 16), lane * 16, dst + 2048);
    const auto rc = RS(ws, ((wave & 1) || (ABL & 2)) ? 0 : WS_END);
    const v4u e4 = {EMPTY, EMPTY, EMPTY, EMPTY};
    asm volatile("buffer_store_dwordx4 %0, %1, %2, %3 offen sc1\n\tbuffer_store_dwordx4 %0, %1, %2, %4 offen sc1\n\t"
                 "buffer_store_dwordx4 %0, %1, %2, %4 offen offset:1024 sc1\n\tbuffer_store_dwordx4 %0, %5, %2, %4 offen offset:2048 sc1\n\ts_nop 1"
                 ::"v"(e4), "v"(lane * 16), "s"(rc), "s"(WS_O + j * O_STRIDE), "s"(WS_F + j * 2304), "v"(lane < 16 ? lane * 16 : 0x7FFF0000) : "memory");
  }
  if (wave == W_C) {
    unsigned long long pc;
    unsigned pcoff;
    asm volatile("s_getpc_b64 %0\n.Lpc%=:\n\ts_mov_b32 %1, .Lpc%=-fused_decode" : "=s"(pc), "=s"(pcoff));
    dma_dev(RS(pc - pcoff, CODE_BYTES), lane < 32 ? lane * 16 + (b >> 3) * 512 : OOB, lds0 + (L_ORED + 1024) * 4);
  }
  const auto rws = RS(ws, WS_END);
  const auto rwq = RS(ws, (ABL & 4) ? 0 : WS_END), rwh = RS(ws, (ABL & 8) ? 0 : WS_END), rwm = RS(ws, (ABL & 16) ? 0 : WS_END), rwp = RS(ws, (ABL & 32) ? 0 : WS_END);

  if (__builtin_expect(wave <= W_F4, 1)) {
   v4u vv[16];
   if (__builtin_expect(wave < 4, 1)) {
    const auto rq = RS(wq, WQ_BYTES), rk = RS(kc, KV_BYTES), rv = RS(vc, KV_BYTES), r1 = RS(w1, W1_BYTES);
    const int qs = (s >> 2) * 144 * 8192 + h * 512 + (s & 3) * 128;
    const int kvb = (h * SK + (SK - WIN) + s * 256) * 256 + wave * 1024, rot = s & 7;
    const int cs = (c8 ^ (r8 & 6)) * 16;
    static_for<0, 43>([&](auto nc) {
      constexpr int n = nc;
      if (n == 5) { __builtin_amdgcn_s_barrier(); CBAR(); }
      if (n >= W) vmwait<W - 1>();
      if constexpr (n >= 3) pace<slot_time(n)>(t0);
      if constexpr (n < 5) {
        dma(rq, min((n * 4 + wave) * 8 + r8, 143) * 8192 + cs, qs, lds0 + L_WQ * 4 + (n * 4 + wave) * 1024);
      } else if constexpr (n >= 11 && n < 27) {
        constexpr int i = n - 11;
        dma(rk, g4 * 256 + ((c ^ (g4 * 4 + (i & 3))) * 16), kvb + ((i + rot) & 15) * 4096, lds0 + L_K * 4 + (wave * 16 + i) * 1024);
      } else if constexpr (n < 11) {
        constexpr int i = n - 5;
        const unsigned v = (unsigned)((rowA + min((i * 4 + wave) * 8 + r8, 161)) * 9216 + cs);
        dma(r1, (int)min(v, v - (unsigned)(W1_BYTES - 128)), lcA * 128, lds0 + L_W1 * 4 + (i * 4 + wave) * 1024);
      } else {
        constexpr int i = n - 27;
        vv[i] = __builtin_amdgcn_raw_buffer_load_b128(rv, lane * 16, kvb + ((i + rot) & 15) * 4096, AUX_NT);
      }
      SB();
      if (n == 0) STAMP(7);
      if (n == 4 + LAG1) { vmwait<LAG1>(); FL[FL_PROG + wave] = 1u; STAMP(1); }
      if (n == 10 + LAG2) { vmwait<LAG2>(); FL[FL_PROG + wave] = 2u; }
      if (n == 26 + LAG3) { vmwait<LAG3>(); FL[FL_PROG + wave] = 3u; STAMP(2); }
      SB();
    });
   } else {
    __builtin_amdgcn_s_barrier();
    CBAR();
#pragma unroll
    for (int i = 0; i < 16; ++i) asm volatile("" : "=v"(vv[i]));
   }
    const int fs = wave;
    const auto ro = RS((const char*)wo + j * 18432, 18432), r2 = RS((const char*)w2 + j * 20736, 20736);
    const bool live = fs < 4 || c < 8;
    const int vo = live ? g4 * 4608 + fs * 256 + c * 16 : OOB, v2 = live ? g4 * 1152 + fs * 256 + c * 16 : OOB - 5 * 4608;
    v4u mskv = keep_mask(c8), mskf = keep_mask(c);
    PIN(mskv); PIN(mskf);
    lds_wait<1>(FL + FL_P + (fs & 3), 1u);
    STAMP(3);
    v4u pk[4];
#pragma unroll
    for (int t = 0; t < 4; ++t) pk[t] = *(LAS v4u*)(L + L_P + (fs & 3) * 128 + ((c >> 3) * 4 + g4) * 16 + t * 4);
    v4u wf[9];
    v4f acc = {0.f, 0.f, 0.f, 0.f};
    static_for<0, 9>([&](auto nc) {
      constexpr int n = nc;
      vmwait<W - 1>();
      pace<slot_time(43 + n)>(t0);
      if constexpr (n < 4) wf[n] = __builtin_amdgcn_raw_buffer_load_b128(ro, vo, n * 1152, AUX_NT);
      else wf[n] = __builtin_amdgcn_raw_buffer_load_b128(r2, v2 + (n - 4) * 4608, 0, AUX_NT);
      SB();
      constexpr int done = n == 0 ? 0 : (n == 1 ? 7 : (n < LAG ? 16 - LAG + n : 16));
      constexpr int ready = n == 0 ? 7 : (n < LAG ? 17 - LAG + n : 16);
#pragma unroll
      for (int i = done; i < ready; ++i) acc = mfma(mskv & pk[i >> 2][i & 3], vv[i], acc);
      if constexpr (n == LAG - 1) {
        *(LAS v4u*)(L + L_ORED + fs * 256 + lane * 4) = box(acc);
        STAMP(4);
      }
      SB();
    });
    int faddr = lane < 32 && live ? WS_F + j * 2304 + (fs * 16 + c) * 32 + g4 * 16 : OOB;
    PIN(faddr);
    lds_wait<1>(FL + FL_H, 1u);
    v4u ha[5];
#pragma unroll
    for (int rg = 0; rg < 5; ++rg) { ha[rg] = mskf & L[L_HS + rg * 4 + g4]; PIN(ha[rg]); }
    int oga = WS_O + (h * 16 + c) * O_STRIDE + s * 64 + g4 * 16;
    PIN(oga);
    lds_wait<0>(FL + FL_OPUB, 1u);
    STAMP(5);
    v4u ov = __builtin_amdgcn_raw_buffer_load_b128(rws, oga, 0, AUX_SC1);
    SB();
    lds_wait<0>(FL + FL_G, 1u);
    float gw = asf(L[L_G + c]);
    PIN(gw);
    v4f a2 = {0.f, 0.f, 0.f, 0.f};
#pragma unroll
    for (int rg = 0; rg < 5; ++rg) a2 = mfma(ha[rg], wf[4 + rg], a2);
    SB();
#pragma nounroll
    for (int sp = 0; sp < SPIN_G; ++sp) {
      COUNT();
      if (__all(full(ov))) break;
      CBAR();
      ov = __builtin_amdgcn_raw_buffer_load_b128(rws, oga, 0, AUX_SC1);
    }
    STAMP(6);
    float u[4];
#pragma unroll
    for (int rg = 0; rg < 4; ++rg) u[rg] = asf(ov[rg]) * gw;
#pragma unroll
    for (int rg = 0; rg < 4; ++rg) u[rg] += dpp<XOR1>(u[rg]);
#pragma unroll
    for (int rg = 0; rg < 4; ++rg) u[rg] += dpp<XOR2>(u[rg]);
#pragma unroll
    for (int rg = 0; rg < 4; ++rg) u[rg] += dpp<XOR7>(u[rg]);
#pragma unroll
    for (int rg = 0; rg < 4; ++rg) u[rg] += dpp<ROR8>(u[rg]);
#pragma unroll
    for (int rg = 0; rg < 4; ++rg) a2 = mfma(mskf & cvtpk(u[rg], u[rg]), wf[rg], a2);
    st128(box(a2), rws, faddr, 0);
    lds_inc(L + L_FLAG + FL_FPUB, lane);
    if (wave != 3) STAMP(7);
  } else if ((wave >> 1) == (W_P >> 1)) {
    const int pw = wave - W_P;
    LAS unsigned* Y = L + L_Y + pw * 512;
    *(LAS v4u*)(Y + lane * 8) = v4u{0u, 0u, 0u, 0u};
    *(LAS v4u*)(Y + lane * 8 + 4) = v4u{0u, 0u, 0u, 0u};
    __builtin_amdgcn_s_barrier();
    CBAR();
    const int nA = min(162, D - rowA), sh = pw == 0 ? 0 : (rowA & 2);
    const int ya = YZ + sh + (pw == 0 ? (s >> 2) * 144 : rowA), yb = pw == 0 ? ya : (nA == 162 ? 0 : YZ + sh - nA);
    const LAS char* ap = (const LAS char*)Y + ((c < 8 ? ya : yb) + g4 * 4) * 2;
    const LAS char* bp = (const LAS char*)(L + (pw == 0 ? L_WQ : L_W1)) + (g4 * 4 + (c >> 2)) * 128 + (c & 3) * 8;
    const int key = (g4 & 1) * 2 + (c >> 3);
    const int gA = pw == 0 ? WS_Q + j * 512 : WS_H + (lcA * 5 + j - (32 * lcA) / 9) * 512;
    int gout = lane < 32 ? gA + lane * 16 : (pw == 1 && nA < 162 ? WS_H + (lcA + 1) * 5 * 512 + (lane - 32) * 16 : OOB);
    PIN(gout);
    vmwait<4>();
    STAMP(1);
    unsigned xp[5];
#pragma unroll
    for (int i = 0; i < 5; ++i) xp[i] = L[L_XR + pw * 768 + i * 64 + lane];
    const unsigned tag = L[L_XR + pw * 768 + 512] + 1u;
#pragma nounroll
    for (int rep = 0; rep <= pw; ++rep) layernorm(xp, lane);
#pragma unroll
    for (int i = 0; i < 5; ++i)
      if (i < 4 || lane < 32) Y[(YZ + sh) / 2 + i * 64 + lane] = xp[i];
    v4u af[6];
#pragma unroll
    for (int ks = 0; ks < 6; ++ks) {
      const v2u lo = *(const LAS v2u*)(ap + ks * 64), hi = *(const LAS v2u*)(ap + ks * 64 + 32);
      af[ks] = v4u{lo.x, lo.y, hi.x, hi.y};
    }
    if (pw == 0) { af[4].z = 0u; af[4].w = 0u; }
    af[5] = v4u{g4 == 0 ? af[5].x : 0u, 0u, 0u, 0u};
#pragma unroll
    for (int ks = 0; ks < 6; ++ks) PIN(af[ks]);
    STAMP(2);
    lds_wait4<1>(FL + FL_PROG, pw + 1u);
    v4f acc[4];
    {
      v4u bf[5][4];
#pragma unroll
      for (int ks = 0; ks < 5; ++ks)
#pragma unroll
        for (int cg = 0; cg < 4; ++cg) {
          const LAS char* q = bp + ((cg ^ key) * 32) + ks * 4096;
          const v2u lo = __builtin_bit_cast(v2u, __builtin_amdgcn_ds_read_tr16_b64_v4i16((LAS v4s*)q));
          const v2u hi = __builtin_bit_cast(v2u, __builtin_amdgcn_ds_read_tr16_b64_v4i16((LAS v4s*)(q + 2048)));
          bf[ks][cg] = v4u{lo.x, lo.y, hi.x, hi.y};
        }
      SB();
#pragma unroll
      for (int cg = 0; cg < 4; ++cg) acc[cg] = v4f{0.f, 0.f, 0.f, 0.f};
#pragma unroll
      for (int ks = 0; ks < 5; ++ks)
#pragma unroll
        for (int cg = 0; cg < 4; ++cg) acc[cg] = mfma(af[ks], bf[ks][cg], acc[cg]);
    }
    if (pw == 1) {
#pragma unroll
      for (int cg = 0; cg < 4; ++cg) {
        const v2u lo = __builtin_bit_cast(v2u, __builtin_amdgcn_ds_read_tr16_b64_v4i16((LAS v4s*)(bp + ((cg ^ key) * 32) + 5 * 4096)));
        acc[cg] = mfma(af[5], v4u{lo.x, lo.y, 0u, 0u}, acc[cg]);
      }
    }
    VM_DRAIN();
    st128(v4u{asu((g4 & 1) ? acc[2][0] : acc[0][0]) & 0xFFFFFF00u, tag, asu((g4 & 1) ? acc[3][0] : acc[1][0]) & 0xFFFFFF00u, tag}, rwp, gout, 0);
    STAMP(3);
    if (pw == 0) {
      pace<SETTLE>(now());
      pace<TQ>(t0);
      v4u g[8];
#pragma nounroll
      for (int sp = 0; sp < SPIN_G; ++sp) {
        CBAR(); COUNT();
#pragma unroll
        for (int i = 0; i < 8; ++i) g[i] = __builtin_amdgcn_raw_buffer_load_b128(rwq, lane * 16 + (i & 3) * 1024, WS_Q + h * 8192 + (i >> 2) * 4096, AUX_SC1);
        unsigned bad = 0, low = 0;
#pragma unroll
        for (int i = 0; i < 8; ++i) { bad |= (g[i].y ^ tag) | (g[i].w ^ tag); low |= g[i].x | g[i].z; }
        if (__all((bad | (low & 0xFFu)) == 0u)) break;
      }
      STAMP(4);
      LAS unsigned short* qd = (LAS unsigned short*)(L + L_QS) + (lane >> 5) * 64 + (g4 & 1) * 32 + c;
#pragma unroll
      for (int u = 0; u < 2; ++u) {
        const float q0 = (asf(g[u].x) + asf(g[2 + u].x)) + (asf(g[4 + u].x) + asf(g[6 + u].x));
        const float q1 = (asf(g[u].z) + asf(g[2 + u].z)) + (asf(g[4 + u].z) + asf(g[6 + u].z));
        const unsigned pk = cvtpk(q0, q1);
        qd[u * 128] = (unsigned short)pk;
        qd[u * 128 + 16] = (unsigned short)(pk >> 16);
      }
      LDS_DRAIN();
      FL[FL_Q] = 1u;
      if (s == 0 && lane < 16) __builtin_amdgcn_raw_buffer_store_b64(v2u{tag, ~tag}, rws, WS_GC + lane * 8, h * 128, AUX_SC1);
    } else {
      const unsigned short bias = __builtin_amdgcn_raw_buffer_load_b16(RS(b1, F * 2), lane < 18 ? lane * 2 + j * 36 : OOB, 0, 0);
      const int f = j * 18 + lane, lc = f >> 6;
      const int cnt = lane < 18 ? (576 * lc + 575) / 162 - (32 * lc) / 9 + 1 : 0;
      const int ha = WS_H + lc * 5 * 512 + (((f >> 5) & 1) * 16 + (f & 15)) * 16 + ((f >> 4) & 1) * 8;
      pace<SETTLE>(now());
      pace<TH>(t0);
      v2u g[5];
#pragma nounroll
      for (int sp = 0; sp < SPIN_G; ++sp) {
        CBAR(); COUNT();
        bool ok = true;
#pragma unroll
        for (int k = 0; k < 5; ++k) g[k] = __builtin_amdgcn_raw_buffer_load_b64(rwh, k < cnt ? ha + k * 512 : OOB, 0, AUX_SC1);
#pragma unroll
        for (int k = 0; k < 5; ++k) ok &= (k >= cnt) | ((g[k].y == tag) & ((g[k].x & 0xFFu) == 0u));
        if (__all(ok)) break;
      }
      STAMP(4);
      const float hsum = ((asf(g[0].x) + asf(g[1].x)) + (asf(g[2].x) + asf(g[3].x))) + asf(g[4].x);
      const float t = rbf(rbf(hsum) + asf((unsigned)bias << 16));
      const float z2 = 2.0f * 0.7978845608028654f * (t + 0.044715f * t * t * t);
      const float ge = t / (1.0f + __builtin_amdgcn_exp2f(-z2 * LOG2E));
      if (lane < 32) L[L_HS + lane] = lane < 18 ? cvtpk(ge, ge) : 0u;
      LDS_DRAIN();
      FL[FL_H] = 1u;
    }
    STAMP(5);
    VM_CLEAR();
  } else if (wave == W_C) {
#pragma unroll
    for (int w = 0; w < 4; ++w) *(LAS v4u*)(L + L_ORED + w * 256 + lane * 4) = v4u{EMPTY, EMPTY, EMPTY, EMPTY};
    LDS_DRAIN();
    __builtin_amdgcn_s_barrier();
    VM_DRAIN();
    STAMP(1);
    lds_wait<4>(FL + FL_FW, 1u);
    const int hh = lane >> 5;
    v4f fw = *(LAS v4f*)(L + L_FW + hh * 4);
    int oaddr = WS_O + j * O_STRIDE + (hh * 128 + c * 8 + (g4 & 1) * 4) * 4;
    PIN(fw); PIN(oaddr);
    pace<slot_time(44)>(t0);
    STAMP(2);
    v4f o;
#pragma nounroll
    for (int sp = 0; sp < SPIN_L; ++sp) {
      CBAR();
      v4u d[4];
#pragma unroll
      for (int w = 0; w < 4; ++w) d[w] = *(LAS v4u*)(L + L_ORED + w * 256 + lane * 4);
      o = __builtin_bit_cast(v4f, d[0]) * fw[0];
#pragma unroll
      for (int w = 1; w < 4; ++w) o += __builtin_bit_cast(v4f, d[w]) * fw[w];
      const float chk = (o[0] + o[1]) + (o[2] + o[3]);
      if (__all(chk == chk)) break;
      if (__all(full(d[0]) & full(d[1]) & full(d[2]) & full(d[3]))) break;
    }
    STAMP(3);
    st128(box(o), rws, oaddr, 0);
    STAMP(4);
    pace<TO>(now());
    FL[FL_OPUB] = 1u;
    VM_CLEAR();
  } else {
    const int aw = wave - W_A;
    if (aw == 3 && lane < 32) L[L_FLAG + lane] = 0u;
    LDS_DRAIN();
    __builtin_amdgcn_s_barrier();
    CBAR();
    const bool reducer = aw == 1 && (j & 31) < 9;
    const int hb = c >> 3;
    pace<slot_time(29)>(t0);
    lds_wait<0>(FL + FL_PROG + aw, 3u);
    STAMP(1);
    v4u kf[16];
#pragma unroll
    for (int t = 0; t < 4; ++t)
#pragma unroll
      for (int kk = 0; kk < 4; ++kk)
        kf[t * 4 + kk] = *(LAS v4u*)(L + L_K + (aw * 16 + t * 4 + (c & 3)) * 256 + (c >> 2) * 64 + (((kk * 4 + g4) ^ c) * 4));
#pragma unroll
    for (int i = 0; i < 16; ++i) PIN(kf[i]);
    lds_wait<0>(FL + FL_Q, 1u);
    STAMP(2);
    v4u qf[4];
#pragma unroll
    for (int kk = 0; kk < 4; ++kk) qf[kk] = *(LAS v4u*)(L + L_QS + hb * 64 + (kk * 4 + g4) * 4);
    SB();
    v4f sc[4];
#pragma unroll
    for (int t = 0; t < 4; ++t) sc[t] = v4f{0.f, 0.f, 0.f, 0.f};
#pragma unroll
    for (int t = 0; t < 4; ++t)
#pragma unroll
      for (int kk = 0; kk < 4; ++kk) sc[t] = mfma(kf[t * 4 + kk], qf[kk], sc[t]);
    float sv[4];
#pragma unroll
    for (int e = 0; e < 4; ++e) {
      const float lo = (c & 1) ? sc[1][e] : sc[0][e], hi = (c & 1) ? sc[3][e] : sc[2][e];
      sv[e] = rbf(rbf((c & 2) ? hi : lo) * 0.08838834764831845f);
    }
    float m = fmaxf(fmaxf(sv[0], sv[1]), fmaxf(sv[2], sv[3]));
    m = fmaxf(m, dpp<XOR1>(m)); m = fmaxf(m, dpp<XOR2>(m));
    m = xrow_max(m);
    float p[4];
    v4u pk;
#pragma unroll
    for (int e = 0; e < 4; ++e) { p[e] = __builtin_amdgcn_exp2f((sv[e] - m) * LOG2E); pk[e] = cvtpk(p[e], p[e]); }
    *(LAS v4u*)(L + L_P + aw * 128 + (hb * 4 + g4) * 16 + (c & 3) * 4) = pk;
    float l = (p[0] + p[1]) + (p[2] + p[3]);
    l += dpp<XOR1>(l); l += dpp<XOR2>(l);
    l = xrow_sum(l);
    if ((lane & 55) == 0) *(LAS v2u*)(L + L_ML + aw * 4 + hb * 2) = v2u{asu(m), asu(l)};
    LDS_DRAIN();
    FL[FL_P + aw] = 1u;
    STAMP(3);
    const unsigned tag = L[L_XR + 512] + 1u;
    if (aw == 0) {
      lds_wait4<0>(FL + FL_P, 1u);
      const int hh = (lane >> 2) & 1, w = lane & 3;
      const v2u ml = *(LAS v2u*)(L + L_ML + w * 4 + hh * 2);
      float M = asf(ml.x);
      M = fmaxf(M, dpp<XOR1>(M)); M = fmaxf(M, dpp<XOR2>(M));
      const float fw = __builtin_amdgcn_exp2f((asf(ml.x) - M) * LOG2E);
      float Ls = asf(ml.y) * fw;
      Ls += dpp<XOR1>(Ls); Ls += dpp<XOR2>(Ls);
      if (lane < 8) L[L_FW + lane] = asu(fw);
      st128(v4u{asu(M) & 0xFFFFFF00u, tag, asu(Ls) & 0xFFFFFF00u, tag}, rws, (lane & 35) == 0 ? WS_O + 1024 + lane * 4 : OOB, j * O_STRIDE);
      LDS_DRAIN();
      FL[FL_FW] = 1u;
      STAMP(4);
      pace<SETTLE>(now());
      pace<TM>(t0);
      v4u g;
#pragma nounroll
      for (int sp = 0; sp < SPIN_G; ++sp) {
        CBAR(); COUNT();
        g = __builtin_amdgcn_raw_buffer_load_b128(rwm, WS_O + 1024 + c * O_STRIDE, h * 16 * O_STRIDE + (s >> 3) * 16, AUX_SC1);
        if (__all(((g.y ^ tag) | (g.w ^ tag) | ((g.x | g.z) & 0xFFu)) == 0u)) break;
      }
      const float Mg = row_max(asf(g.x));
      const float gw = __builtin_amdgcn_exp2f((asf(g.x) - Mg) * LOG2E);
      const float Lg = row_sum(asf(g.z) * gw);
      if (lane < 16) L[L_G + lane] = asu(gw * __builtin_amdgcn_rcpf(Lg));
      LDS_DRAIN();
      FL[FL_G] = 1u;
      STAMP(5);
    } else if (reducer) {
      const int red = (j >> 5) * 9 + (j & 31);
      const int ocol = red * 8 + (lane & 1) * 4 + g4;
      const unsigned xb = ((LAS unsigned short*)(L + L_XR))[ocol];
      const unsigned bb = __builtin_amdgcn_raw_buffer_load_b16(RS(b2, D * 2), ocol * 2, 0, 0);
      float base = asf(xb << 16) + asf(bb << 16);
      PIN(base);
      pace<TM>(t0);
#pragma nounroll
      for (int sp = 0; sp < SPIN_G; ++sp) {
        CBAR();
        const v2u cf = __builtin_amdgcn_raw_buffer_load_b64(rws, WS_GC + c * 128, 0, AUX_SC1);
        if (__all((cf.x == tag) & (cf.y == ~tag))) break;
        __builtin_amdgcn_s_sleep(8);
      }
      lds_wait<1>(FL + FL_FPUB, 5u);
      __builtin_amdgcn_s_sleep(FDELAY);
      STAMP(4);
      v4f sum;
      bool done = false;
#pragma nounroll
      for (int sp = 0; sp < SPIN_G; ++sp) {
        CBAR(); COUNT();
        if (!done) {
          v4u pv[8];
#pragma unroll
          for (int i = 0; i < 8; ++i) pv[i] = __builtin_amdgcn_raw_buffer_load_b128(rws, (lane >> 1) * 2304 + (lane & 1) * 16, WS_F + red * 32 + i * 32 * 2304, AUX_SC1);
          unsigned mx = 0;
          sum = __builtin_bit_cast(v4f, pv[0]);
#pragma unroll
          for (int i = 1; i < 8; ++i) sum += __builtin_bit_cast(v4f, pv[i]);
          done = true;
          const float chk = (sum[0] + sum[1]) + (sum[2] + sum[3]);
          if (chk != chk) {
#pragma unroll
            for (int i = 0; i < 8; ++i) mx = max(max(mx, max(pv[i].x, pv[i].y)), max(pv[i].z, pv[i].w));
            done = mx != EMPTY;
          }
        }
        if (__all(done)) break;
      }
      STAMP(5);
      auto s02 = __builtin_amdgcn_permlane32_swap(asu(sum[0]), asu(sum[2]), false, false);
      auto s13 = __builtin_amdgcn_permlane32_swap(asu(sum[1]), asu(sum[3]), false, false);
      const float u0 = asf(s02[0]) + asf(s02[1]), u1 = asf(s13[0]) + asf(s13[1]);
      auto s01 = __builtin_amdgcn_permlane16_swap(asu(u0), asu(u1), false, false);
      float u = asf(s01[0]) + asf(s01[1]);
      u += dpp<ROR8>(u); u += dpp<ROR4>(u); u += dpp<XOR2>(u);
      u += base;
      if ((lane & 14) == 0) __builtin_amdgcn_raw_buffer_store_b16((unsigned short)cvtpk(u, u), RS(out, D * 2), ocol * 2, 0, 0);
      if (j == 0 && lane == 0) __builtin_amdgcn_raw_buffer_store_b32(tag, rws, 0, 0, AUX_SC1);
      STAMP(6);
    }
    VM_CLEAR();
  }

}

extern "C" void kernel_launch(void* const* d_in, const int* in_sizes, int n_in, void* d_out, int out_size, void* d_ws, size_t ws_size,
                              hipStream_t stream) {
  fused_decode<<<256, NWAVE * 64, 0, stream>>>(d_in[3], d_in[1], d_in[6], d_in[2], d_in[4], d_in[8], d_ws, d_in[0], d_in[7], d_in[9], d_out);
}
```

```cpp
#include <hip/hip_runtime.h>
#include <stdint.h>
#include <type_traits>

typedef unsigned v4u __attribute__((ext_vector_type(4)));
typedef unsigned v2u __attribute__((ext_vector_type(2)));
typedef float v4f __attribute__((ext_vector_type(4)));
typedef float v2f __attribute__((ext_vector_type(2)));
typedef short v4s __attribute__((ext_vector_type(4)));
typedef __bf16 bf16x8 __attribute__((ext_vector_type(8)));
typedef __bf16 bf16x2 __attribute__((ext_vector_type(2)));
#define LAS __attribute__((address_space(3)))

constexpr int D = 576, SK = 16384, WIN = 4096, F = 4608;
constexpr int KV_BYTES = 16 * SK * 128 * 2;
constexpr int WQ_BYTES = D * 4096 * 2, W1_BYTES = D * F * 2;
constexpr int WS_TT = 4096  , WS_Q = 8192, WS_H = WS_Q + 256 * 256, WS_O = WS_H + 72 * 5 * 256, O_STRIDE = 1152,
              WS_F = WS_O + 256 * O_STRIDE, F_STRIDE = 256 * 64 + 256, WS_END = WS_F + 36 * F_STRIDE;
constexpr unsigned EMPTY = 0xFFFFFFFFu;
constexpr int OOB = 0x7FFFFFFF;
#define WINDOW 11
#define PACE10 146
#define PACE0 80
#define BLIND 3
#define AHEAD 6
#define LAG 4
#define LAG1 4
#define LAG2 7
#define LAG3 5
#define TQ (slot_time(27))
#define TH (slot_time(31))
#define TM (slot_time(48))
#define EARLY AHEAD
#define DRIFT0 29
#define TS (slot_time(22))
#define SYNC 1
#define OATOM 0
#define TOUCH 1
#define EXTRA 30
#define STAG 0
#define PV0 42
#define TG 15
#define F4S 43
#define F4D 1
#define F4CLR VM_CLEAR()
#define FCOST 100
#define SETTLE 60
#define TO 45
#define TF 25
constexpr int AUX_NT = 2, AUX_SC1 = 16, W = WINDOW;
#define ABL 0
constexpr int SPIN_G = ABL ? 1 : 20000, SPIN_L = 400000;
constexpr float LOG2E = 1.4426950408889634f;

constexpr int L_WQ = 0  , L_K = 5120  , L_W1 = L_K + 16384  , L_FLAG = L_W1 + 6144  ;
constexpr int FL_Q = 0, FL_H = 1, FL_PROG = 4  , FL_P = 8  , FL_KDONE = 13, FL_FW = 14, FL_G = 15, FL_VDONE = 16, FL_OPUB = 17,
              FL_FPUB = 18, FL_T0 = 20  , FL_T1 = 22  ;
constexpr int L_XR = L_FLAG + 32  , L_Y = L_XR + 2 * 1024  , L_QS = L_Y + 2 * 512  , L_HS = L_QS + 128  ,
              L_ML = L_HS + 32  , L_FW = L_ML + 16  , L_G = L_FW + 16  , L_OS = L_G + 16  , L_ORED = L_OS + 16  ,
              L_P = L_ORED + 1280  , L_END = L_P + 512;
constexpr int YZ = 192;

#define RS(p, n) __builtin_amdgcn_make_buffer_rsrc((void*)(p), 0, (int)(n), 0x00020000)
#define SB() __builtin_amdgcn_sched_barrier(0)
#define CBAR() asm volatile("" ::: "memory")
#define LDS_DRAIN() asm volatile("s_waitcnt lgkmcnt(0)" ::: "memory")
#define VM_DRAIN() asm volatile("s_waitcnt vmcnt(0)" ::: "memory")
#define VM_CLEAR() __builtin_amdgcn_s_waitcnt(0x0F70)
#define PIN(x) asm volatile("" : "+v"(x))

#define COUNT() do {} while (0)
#define STAMP(i) do {} while (0)

__device__ __forceinline__ float asf(unsigned u) { return __builtin_bit_cast(float, u); }
__device__ __forceinline__ unsigned asu(float f) { return __builtin_bit_cast(unsigned, f); }
__device__ __forceinline__ unsigned cvtpk(float lo, float hi) {
  return __builtin_bit_cast(unsigned, __builtin_convertvector(v2f{lo, hi}, bf16x2));
}
__device__ __forceinline__ v2f unpk(unsigned p) { return v2f{asf(p << 16), asf(p & 0xFFFF0000u)}; }
__device__ __forceinline__ float rbf(float f) { return asf(cvtpk(0.f, f)); }
__device__ __forceinline__ float vmax(float a, float b) { float r; asm("v_max_f32 %0, %1, %2" : "=v"(r) : "v"(a), "v"(b)); return r; }
__device__ __forceinline__ float vmax3(float a, float b, float c) { float r; asm("v_max3_f32 %0, %1, %2, %3" : "=v"(r) : "v"(a), "v"(b), "v"(c)); return r; }
template <int CTRL> __device__ __forceinline__ float dpp(float x) {
  return asf(__builtin_amdgcn_mov_dpp(asu(x), CTRL, 0xf, 0xf, true));
}
constexpr int XOR1 = 0xB1, XOR2 = 0x4E, XOR7 = 0x141, ROR8 = 0x128, ROR4 = 0x124;
__device__ __forceinline__ float xrow_sum(float x) {
  auto s = __builtin_amdgcn_permlane16_swap(asu(x), asu(x), false, false);
  x = asf(s[0]) + asf(s[1]);
  auto t = __builtin_amdgcn_permlane32_swap(asu(x), asu(x), false, false);
  return asf(t[0]) + asf(t[1]);
}
__device__ __forceinline__ float xrow_max(float x) {
  auto s = __builtin_amdgcn_permlane16_swap(asu(x), asu(x), false, false);
  x = fmaxf(asf(s[0]), asf(s[1]));
  auto t = __builtin_amdgcn_permlane32_swap(asu(x), asu(x), false, false);
  return fmaxf(asf(t[0]), asf(t[1]));
}
__device__ __forceinline__ float row_sum(float x) {
  x += dpp<XOR1>(x); x += dpp<XOR2>(x); x += dpp<XOR7>(x); x += dpp<ROR8>(x);
  return x;
}
__device__ __forceinline__ float row_max(float x) {
  asm("s_nop 1\n\tv_max_f32_dpp %0, %0, %0 quad_perm:[1,0,3,2] row_mask:0xf bank_mask:0xf\n\ts_nop 1\n\t"
      "v_max_f32_dpp %0, %0, %0 quad_perm:[2,3,0,1] row_mask:0xf bank_mask:0xf\n\ts_nop 1\n\t"
      "v_max_f32_dpp %0, %0, %0 row_half_mirror row_mask:0xf bank_mask:0xf\n\ts_nop 1\n\t"
      "v_max_f32_dpp %0, %0, %0 row_ror:8 row_mask:0xf bank_mask:0xf" : "+v"(x));
  return x;
}
__device__ __forceinline__ v4f mfma(v4u a, v4u b, v4f c) {
  return __builtin_amdgcn_mfma_f32_16x16x32_bf16(__builtin_bit_cast(bf16x8, a), __builtin_bit_cast(bf16x8, b), c, 0, 0, 0);
}
__device__ __forceinline__ v4u box(v4f v) {
  v4u r;
#pragma unroll
  for (int i = 0; i < 4; ++i) r[i] = min(asu(v[i]), 0xFFFFFFFEu);
  return r;
}
constexpr unsigned TAG_MASK = 0x3FFu;
__device__ __forceinline__ v4u seal(v4f v, unsigned tag) {
  v4u r;
#pragma unroll
  for (int i = 0; i < 4; ++i) r[i] = (asu(v[i]) & ~TAG_MASK) | tag;
  return r;
}
__device__ __forceinline__ v4f unseal(v4u w, unsigned tag, unsigned& bad) {
  const v4u x = w ^ tag;
  bad |= (x.x | x.y) | (x.z | x.w);
  return __builtin_bit_cast(v4f, x);
}
__device__ __forceinline__ bool full(v4u v) { return max(max(v.x, v.y), max(v.z, v.w)) != EMPTY; }
template <int NAP = 2> __device__ __forceinline__ void lds_wait(volatile LAS unsigned* f, unsigned need) {
#pragma nounroll
  for (int sp = 0; sp < SPIN_L; ++sp) {
    if ((unsigned)__builtin_amdgcn_readfirstlane(*f) >= need) break;
    if (NAP) __builtin_amdgcn_s_sleep(NAP);
  }
  CBAR();
}
template <int NAP> __device__ __forceinline__ void lds_wait4(volatile LAS unsigned* f, unsigned need) {
#pragma nounroll
  for (int sp = 0; sp < SPIN_L; ++sp) {
    const v4u v = *(volatile LAS v4u*)f;
    if ((unsigned)__builtin_amdgcn_readfirstlane(min(min(v.x, v.y), min(v.z, v.w))) >= need) break;
    if (NAP) __builtin_amdgcn_s_sleep(NAP);
  }
  CBAR();
}
__device__ __forceinline__ void lds_inc(LAS unsigned* f, int lane) {
  if (lane == 0) __hip_atomic_fetch_add(f, 1u, __ATOMIC_RELAXED, __HIP_MEMORY_SCOPE_WORKGROUP);
}
typedef __attribute__((address_space(1))) unsigned long long gu64;
__device__ __forceinline__ v2u peek(void* ws, int off) {
  unsigned long long zero = 0ull;
  asm volatile("" : "+v"(zero));
  const unsigned long long r = __hip_atomic_fetch_or((gu64*)((char*)ws + off), zero, __ATOMIC_RELAXED, __HIP_MEMORY_SCOPE_AGENT);
  return v2u{(unsigned)r, (unsigned)(r >> 32)};
}
__device__ __forceinline__ void touch(const void* p) {
  unsigned d;
  asm volatile("s_load_dword %0, %1, 0x0\n\ts_waitcnt lgkmcnt(0)" : "=s"(d) : "s"(p) : "memory");
}
__device__ __forceinline__ unsigned now() {
  unsigned long long t;
  asm volatile("s_memrealtime %0\n\ts_waitcnt lgkmcnt(0)" : "=s"(t)::"memory");
  return (unsigned)t;
}
template <int T> __device__ __forceinline__ void pace(unsigned t0) {
  asm volatile("1:\n\ts_memrealtime s[90:91]\n\ts_waitcnt lgkmcnt(0)\n\ts_sub_u32 s90, s90, %0\n\ts_cmpk_lt_u32 s90, %1\n\ts_cbranch_scc1 1b"
               ::"s"(__builtin_amdgcn_readfirstlane(t0)), "i"(T) : "memory", "scc", "s90", "s91");
}
constexpr int slot_cost(int n) { return n == 10 ? 30 : (n >= 43 ? (n == 47 ? 60 : FCOST) : 100); }
constexpr int slot_time(int n) {
  if (n < AHEAD) return (n - BLIND + 1) * 15;
  int c = 0;
  for (int i = AHEAD; i < n; ++i) c += slot_cost(i);
  return PACE0 + c * PACE10 / 1000;
}
constexpr int pv_done(int n) { return n < PV0 ? 0 : ((n - PV0) * 5 + 2 < n - 26 - LAG ? (n - PV0) * 5 + 2 : n - 26 - LAG); }
template <int N> __device__ __forceinline__ void vmwait() { asm volatile("s_waitcnt vmcnt(%0)" ::"i"(N) : "memory"); }
__device__ __forceinline__ void st128(v4u d, __amdgpu_buffer_rsrc_t r, int voff, int soff) {
  __builtin_amdgcn_raw_buffer_store_b128(d, r, voff, soff, AUX_SC1);
  asm volatile("s_nop 1" ::"v"(d) : "memory");
}
__device__ __forceinline__ void dma(__amdgpu_buffer_rsrc_t r, int voff, int soff, int ldsaddr) {
  asm volatile("s_mov_b32 m0, %0\n\ts_nop 0\n\tbuffer_load_dwordx4 %1, %2, %3 offen nt lds" ::"s"(ldsaddr), "v"(voff), "s"(r), "s"(soff) : "memory");
}
__device__ __forceinline__ void dma_dev(__amdgpu_buffer_rsrc_t r, int voff, int ldsaddr) {
  asm volatile("s_mov_b32 m0, %0\n\ts_nop 0\n\tbuffer_load_dwordx4 %1, %2, 0 offen sc1 lds" ::"s"(ldsaddr), "v"(voff), "s"(r) : "memory");
}
__device__ __forceinline__ v4u keep_mask(int m) {
  v4u k;
#pragma unroll
  for (int v = 0; v < 4; ++v) k[v] = ((m >> 1) == v) ? ((m & 1) ? 0xFFFF0000u : 0x0000FFFFu) : 0u;
  return k;
}
template <int I, int N, class Fn> __device__ __forceinline__ void static_for(Fn&& fn) {
  if constexpr (I < N) { fn(std::integral_constant<int, I>{}); static_for<I + 1, N>(fn); }
}
__device__ __forceinline__ void layernorm(unsigned (&p)[5], int lane) {
  const float c0 = asf(__builtin_amdgcn_readfirstlane(p[0]) << 16);
  v2f v[5], d[5];
#pragma unroll
  for (int i = 0; i < 5; ++i) { v[i] = unpk(p[i]); d[i] = v[i] - c0; }
  if (lane >= 32) d[4] = v2f{0.f, 0.f};
  const v2f s1v = (d[0] + d[1]) + (d[2] + d[3]) + d[4];
  v2f s2v = d[0] * d[0];
#pragma unroll
  for (int i = 1; i < 5; ++i) s2v = __builtin_elementwise_fma(d[i], d[i], s2v);
  const float s1 = xrow_sum(row_sum(s1v.x + s1v.y)) * (1.0f / D), s2 = xrow_sum(row_sum(s2v.x + s2v.y)) * (1.0f / D);
  const float var = fmaxf(fmaf(-s1, s1, s2), 0.f);
  const float mb = rbf(c0 + s1), sd = rbf(__builtin_amdgcn_sqrtf(rbf(rbf(var) + 1e-5f)));
  const float inv = __builtin_amdgcn_rcpf(sd);
#pragma unroll
  for (int i = 0; i < 5; ++i) {
    const v2f t = v[i] - mb;
    const v2f u = v2f{rbf(t.x), rbf(t.y)} * inv;
    p[i] = cvtpk(u.x, u.y);
  }
}

constexpr int W_F4 = 4, W_C = 5, W_P = 6, W_A = 8, NWAVE = 12;

extern "C" __global__ void __launch_bounds__(NWAVE * 64, 3)
fused_decode(const void* __restrict__ wq, const void* __restrict__ kc, const void* __restrict__ w1, const void* __restrict__ vc,
             const void* __restrict__ wo, const void* __restrict__ w2, void* __restrict__ ws, const void* __restrict__ x,
             const void* __restrict__ b1, const void* __restrict__ b2, void* __restrict__ out) {
  __shared__ __attribute__((aligned(16))) unsigned smem[L_END];
  LAS unsigned* L = (LAS unsigned*)smem;
  volatile LAS unsigned* FL = (volatile LAS unsigned*)smem + L_FLAG;
  const int tid = threadIdx.x, lane = tid & 63, wave = __builtin_amdgcn_readfirstlane(tid >> 6);
  const int b = blockIdx.x, j = (b & 7) * 32 + (b >> 3), h = j >> 4, s = j & 15;
  const int r8 = lane >> 3, c8 = lane & 7, g4 = lane >> 4, c = lane & 15;
  const int lcA = (j * 9) >> 5, rowA = 18 * ((j * 9) & 31);
  const int lds0 = (int)(unsigned)(uintptr_t)L;
  if ((wave >> 1) == (W_P >> 1)) {
    const int dst = lds0 + (L_XR + (wave & 1) * 1024) * 4;
    dma_dev(RS(x, D * 2), lane * 16, dst);
    dma_dev(RS(x, D * 2), lane * 16 + 1024, dst + 1024);
    dma_dev(RS(ws, 16), lane * 16, dst + 2048);
    const int red8 = ((j >> 5) * 9 + min(j & 31, 8)) * 16;
    dma_dev(RS((wave & 1) ? (const char*)b1 : (const char*)b2, (wave & 1) ? F * 2 : D * 2), lane < 4 ? lane * 16 + ((wave & 1) ? (j * 36) & ~15 : red8) : OOB, dst + 3072);
  }
  if (wave == W_C) {
    unsigned long long pc;
    unsigned pcoff, len;
    asm volatile("s_getpc_b64 %0\n.Lpc%=:\n\ts_mov_b32 %1, .Lpc%=-fused_decode\n\ts_mov_b32 %2, fused_decode_end-fused_decode"
                 : "=s"(pc), "=s"(pcoff), "=s"(len));
    dma_dev(RS(pc - pcoff, len), lane < 40 ? lane * 16 + (b >> 3) * 640 : OOB, lds0 + (L_ORED + 1024) * 4);
  }
  const auto rws = RS(ws, WS_END);
  const auto rwq = RS(ws, (ABL & 4) ? 0 : WS_END), rwh = RS(ws, (ABL & 8) ? 0 : WS_END), rwm = RS(ws, (ABL & 16) ? 0 : WS_END), rwp = RS(ws, (ABL & 32) ? 0 : WS_END);

  if (__builtin_expect(wave <= W_F4, 1)) {
    v4u wf[9], ha[5], mskf;
    int vo, v2, faddr, oga, mga;
    unsigned tag;
    auto prep_addr = [&]() {
      const bool live = wave < 4 || c < 8;
      vo = live ? g4 * 4608 + wave * 256 + c * 16 : OOB;
      v2 = live ? g4 * 1152 + wave * 256 + c * 16 : OOB - 5 * 4608;
      faddr = lane < 32 && live ? WS_F + (wave * 8 + (c >> 1)) * F_STRIDE + j * 64 + (c & 1) * 32 + g4 * 16 : OOB;
      oga = WS_O + (h * 16 + c) * O_STRIDE + s * 64 + g4 * 16;
      mga = WS_O + 1024 + (h * 16 + c) * O_STRIDE + (s >> 3) * 16;
      mskf = keep_mask(c);
      PIN(mga); PIN(vo); PIN(v2); PIN(faddr); PIN(oga); PIN(mskf);
    };
    auto prep_h = [&]() {
      lds_wait<0>(FL + FL_H, 1u);
      tag = (L[L_XR + 512] + 1u) & TAG_MASK;
      PIN(tag);
#pragma unroll
      for (int rg = 0; rg < 5; ++rg) { ha[rg] = mskf & L[L_HS + rg * 4 + g4]; PIN(ha[rg]); }
    };
    if (__builtin_expect(wave < 4, 1)) {
      const auto rq = RS(wq, WQ_BYTES), rk = RS(kc, KV_BYTES), rv = RS(vc, KV_BYTES), r1 = RS(w1, W1_BYTES);
      const int qs = (s >> 2) * 144 * 8192 + h * 512 + (s & 3) * 128;
      const int kvb = (h * SK + (SK - WIN) + s * 256) * 256 + wave * 1024, rot = s & 7;
      const int cs = (c8 ^ (r8 & 6)) * 16;
      __amdgpu_buffer_rsrc_t ro, r2;
      v4u vv[16], pk[4], mskv, alast;
      v4f acc = {0.f, 0.f, 0.f, 0.f};
      unsigned t0;
      int drift = 0;
      static_for<0, 52>([&](auto nc) {
        constexpr int n = nc;
        if (n == 1) t0 = now();
        if (n == 5) __builtin_amdgcn_s_barrier();
        if (n == AHEAD) {
          vmwait<AHEAD - 1>();
          const unsigned t1 = now() - slot_time(n);
          if ((int)(t1 - t0) > 0) t0 = t1 + min((int)(t1 - t0), EXTRA);
          if (wave == 0) *(volatile LAS v2u*)(FL + FL_T0) = v2u{t0, 1u};
        }
        if (n >= W) vmwait<W - 1>();
        if constexpr (n >= 43) pace<slot_time(n)>(t0 + drift);
        else if constexpr (n >= DRIFT0) pace<slot_time(n)>(t0 + ((drift * ((n - DRIFT0 + 1) * 256 / (43 - DRIFT0))) >> 8));
        else if constexpr (n >= BLIND) pace<slot_time(n)>(t0);
        if constexpr (n < 5) {
          dma(rq, min((n * 4 + wave) * 8 + r8, 143) * 8192 + cs, qs, lds0 + L_WQ * 4 + (n * 4 + wave) * 1024);
        } else if constexpr (n < 11) {
          constexpr int i = n - 5;
          const unsigned v = (unsigned)((rowA + min((i * 4 + wave) * 8 + r8, 161)) * 9216 + cs);
          dma(r1, (int)min(v, v - (unsigned)(W1_BYTES - 128)), lcA * 128, lds0 + L_W1 * 4 + (i * 4 + wave) * 1024);
        } else if constexpr (n < 27) {
          constexpr int i = n - 11;
          dma(rk, g4 * 256 + ((c ^ (g4 * 4 + (i & 3))) * 16), kvb + ((i + rot) & 15) * 4096, lds0 + L_K * 4 + (wave * 16 + i) * 1024);
        } else if constexpr (n < 43) {
          constexpr int i = n - 27;
          vv[i] = __builtin_amdgcn_raw_buffer_load_b128(rv, lane * 16, kvb + ((i + rot) & 15) * 4096, AUX_NT);
        } else if constexpr (n < 48) {
          wf[n - 43] = __builtin_amdgcn_raw_buffer_load_b128(r2, v2 + (n - 43) * 4608, 0, AUX_NT);
        } else {
          wf[n - 43] = __builtin_amdgcn_raw_buffer_load_b128(ro, vo, (n - 48) * 1152, AUX_NT);
        }
        SB();
        if (n == AHEAD) STAMP(7);
        if (n == 4 + LAG1) { vmwait<LAG1>(); FL[FL_PROG + wave] = 1u; STAMP(1); }
        if (n == 10 + LAG2) { vmwait<LAG2>(); FL[FL_PROG + wave] = 2u; }
        if (n == 26 + LAG3) { vmwait<LAG3>(); FL[FL_PROG + wave] = 3u; STAMP(2); }
        if (n == 21) prep_addr();
        if (n == 22) {
          mskv = keep_mask(c8);
          PIN(mskv);
          ro = RS((const char*)wo + j * 18432, 18432);
          r2 = RS((const char*)w2 + j * 20736, 20736);
          asm volatile("" : "+s"(ro), "+s"(r2));
        }
        if (n == DRIFT0 - 1 || n == 36) {
          const v2u t1 = *(volatile LAS v2u*)(FL + FL_T1);
          drift = __builtin_amdgcn_readfirstlane(t1.y) ? (int)(__builtin_amdgcn_readfirstlane(t1.x) - t0) : 0;
        }
        if (n == PV0) {
          lds_wait<0>(FL + FL_P + wave, 1u);
          STAMP(3);
#pragma unroll
          for (int t = 0; t < 4; ++t) pk[t] = *(LAS v4u*)(L + L_P + wave * 128 + ((c >> 3) * 4 + g4) * 16 + t * 4);
          alast = mskv & pk[3][3];
          PIN(alast);
        }
        if constexpr (n >= PV0 && n <= 42 + LAG) {
          constexpr int done = pv_done(n - 1), ready = pv_done(n);
#pragma unroll
          for (int i = done; i < ready; ++i) acc = mfma(i == 15 ? alast : mskv & pk[i >> 2][i & 3], vv[i], acc);
          if (n == 42 + LAG) {
            *(LAS v4u*)(L + L_ORED + wave * 256 + lane * 4) = box(acc);
            STAMP(4);
          }
        }
        if (n == 48) prep_h();
        SB();
      });
    } else {
      __builtin_amdgcn_s_barrier();
      prep_addr();
      const auto ro = RS((const char*)wo + j * 18432, 18432), r2 = RS((const char*)w2 + j * 20736, 20736);
      lds_wait<4>(FL + FL_T1 + 1, 1u);
      const unsigned t0 = __builtin_amdgcn_readfirstlane(FL[FL_T1]);
      static_for<0, 9>([&](auto nc) {
        constexpr int n = nc;
        pace<slot_time(F4S + n * F4D)>(t0);
        if constexpr (n < 5) wf[n] = __builtin_amdgcn_raw_buffer_load_b128(r2, v2 + n * 4608, 0, AUX_NT);
        else wf[n] = __builtin_amdgcn_raw_buffer_load_b128(ro, vo, (n - 5) * 1152, AUX_NT);
        SB();
      });
      prep_h();
      F4CLR;
    }
    lds_wait<0>(FL + FL_OPUB, 1u);
    STAMP(5);
    v4u mw = __builtin_amdgcn_raw_buffer_load_b128(rws, mga, 0, AUX_SC1), ow = __builtin_amdgcn_raw_buffer_load_b128(rws, oga, 0, AUX_SC1);
    SB();
    v4f a2 = {0.f, 0.f, 0.f, 0.f};
#pragma unroll
    for (int rg = 0; rg < 5; ++rg) a2 = mfma(ha[rg], wf[rg], a2);
    PIN(a2);
    v4f u, ml;
#pragma nounroll
    for (int sp = 0; sp < SPIN_G; ++sp) {
      COUNT();
      unsigned bad = 0;
      ml = unseal(mw, tag, bad);
      u = unseal(ow, tag, bad);
      if (__all((bad & TAG_MASK) == 0u)) break;
      CBAR();
      mw = __builtin_amdgcn_raw_buffer_load_b128(rws, mga, 0, AUX_SC1);
      ow = __builtin_amdgcn_raw_buffer_load_b128(rws, oga, 0, AUX_SC1);
    }
    STAMP(6);
    const float gw = __builtin_amdgcn_exp2f((ml[0] - row_max(ml[0])) * LOG2E);
    float r[5] = {u[0] * gw, u[1] * gw, u[2] * gw, u[3] * gw, ml[1] * gw};
#pragma unroll
    for (int i = 4; i >= 0; --i) r[i] += dpp<XOR1>(r[i]);
#pragma unroll
    for (int i = 4; i >= 0; --i) r[i] += dpp<XOR2>(r[i]);
#pragma unroll
    for (int i = 4; i >= 0; --i) r[i] += dpp<XOR7>(r[i]);
#pragma unroll
    for (int i = 4; i >= 0; --i) r[i] += dpp<ROR8>(r[i]);
    const float inv = __builtin_amdgcn_rcpf(r[4]);
#pragma unroll
    for (int rg = 0; rg < 4; ++rg) a2 = mfma(mskf & cvtpk(r[rg] * inv, r[rg] * inv), wf[5 + rg], a2);
    st128(seal(a2, tag), rws, faddr, 0);
    lds_inc(L + L_FLAG + FL_FPUB, lane);
    if (wave != 3) STAMP(7);
  } else if ((wave >> 1) == (W_P >> 1)) {
    const int pw = wave - W_P;
    LAS unsigned* Y = L + L_Y + pw * 512;
    *(LAS v4u*)(Y + lane * 8) = v4u{0u, 0u, 0u, 0u};
    *(LAS v4u*)(Y + lane * 8 + 4) = v4u{0u, 0u, 0u, 0u};
    __builtin_amdgcn_s_barrier();
    CBAR();
    const int nA = min(162, D - rowA), sh = pw == 0 ? 0 : (rowA & 2);
    const int ya = YZ + sh + (pw == 0 ? (s >> 2) * 144 : rowA), yb = pw == 0 ? ya : (nA == 162 ? 0 : YZ + sh - nA);
    const LAS char* ap = (const LAS char*)Y + ((c < 8 ? ya : yb) + g4 * 4) * 2;
    const LAS char* bp = (const LAS char*)(L + (pw == 0 ? L_WQ : L_W1)) + (g4 * 4 + (c >> 2)) * 128 + (c & 3) * 8;
    const int key = (g4 & 1) * 2 + (c >> 3);
    const int gA = pw == 0 ? WS_Q + j * 256 : WS_H + (lcA * 5 + j - (32 * lcA) / 9) * 256;
    int gout = g4 == 0 ? gA + c * 16 : (g4 == 2 && pw == 1 && nA < 162 ? WS_H + (lcA + 1) * 5 * 256 + c * 16 : OOB);
    PIN(gout);
    VM_DRAIN();
    STAMP(1);
    unsigned xp[5];
#pragma unroll
    for (int i = 0; i < 5; ++i) xp[i] = L[L_XR + pw * 1024 + i * 64 + lane];
    const unsigned tag = (L[L_XR + pw * 1024 + 512] + 1u) & TAG_MASK;
#pragma nounroll
    for (int rep = 0; rep <= pw; ++rep) layernorm(xp, lane);
#pragma unroll
    for (int i = 0; i < 5; ++i)
      if (i < 4 || lane < 32) Y[(YZ + sh) / 2 + i * 64 + lane] = xp[i];
    v4u af[6];
#pragma unroll
    for (int ks = 0; ks < 6; ++ks) {
      const v2u lo = *(const LAS v2u*)(ap + ks * 64), hi = *(const LAS v2u*)(ap + ks * 64 + 32);
      af[ks] = v4u{lo.x, lo.y, hi.x, hi.y};
    }
    if (pw == 0) { af[4].z = 0u; af[4].w = 0u; }
    af[5] = v4u{g4 == 0 ? af[5].x : 0u, 0u, 0u, 0u};
#pragma unroll
    for (int ks = 0; ks < 6; ++ks) PIN(af[ks]);
    STAMP(2);
    lds_wait4<1>(FL + FL_PROG, pw + 1u);
    v4f acc[4];
    {
      v4u bf[5][4];
#pragma unroll
      for (int ks = 0; ks < 5; ++ks)
#pragma unroll
        for (int cg = 0; cg < 4; ++cg) {
          const LAS char* q = bp + ((cg ^ key) * 32) + ks * 4096;
          const v2u lo = __builtin_bit_cast(v2u, __builtin_amdgcn_ds_read_tr16_b64_v4i16((LAS v4s*)q));
          const v2u hi = __builtin_bit_cast(v2u, __builtin_amdgcn_ds_read_tr16_b64_v4i16((LAS v4s*)(q + 2048)));
          bf[ks][cg] = v4u{lo.x, lo.y, hi.x, hi.y};
        }
      SB();
#pragma unroll
      for (int cg = 0; cg < 4; ++cg) acc[cg] = v4f{0.f, 0.f, 0.f, 0.f};
#pragma unroll
      for (int ks = 0; ks < 5; ++ks)
#pragma unroll
        for (int cg = 0; cg < 4; ++cg) acc[cg] = mfma(af[ks], bf[ks][cg], acc[cg]);
    }
    if (pw == 1) {
#pragma unroll
      for (int cg = 0; cg < 4; ++cg) {
        const v2u lo = __builtin_bit_cast(v2u, __builtin_amdgcn_ds_read_tr16_b64_v4i16((LAS v4s*)(bp + ((cg ^ key) * 32) + 5 * 4096)));
        acc[cg] = mfma(af[5], v4u{lo.x, lo.y, 0u, 0u}, acc[cg]);
      }
    }
    for (int i = 0; i < STAG * s; ++i) __builtin_amdgcn_s_sleep(1);
    st128(seal(v4f{acc[0][0], acc[1][0], acc[2][0], acc[3][0]}, tag), rwp, gout, 0);
    STAMP(3);
    const unsigned tpub = now();
    lds_wait<2>(FL + FL_T0 + 1, 1u);
    const unsigned t0 = __builtin_amdgcn_readfirstlane(FL[FL_T0]);
    if (pw == 0) {
      pace<SETTLE>(tpub);
      pace<TQ>(t0);
      v4f q;
#pragma nounroll
      for (int sp = 0; sp < SPIN_G; ++sp) {
        CBAR(); COUNT();
        v4u g[4];
#pragma unroll
        for (int i = 0; i < 4; ++i) g[i] = __builtin_amdgcn_raw_buffer_load_b128(rwq, lane * 16 + i * 1024, WS_Q + h * 4096, AUX_SC1);
        unsigned bad = 0;
        q = (unseal(g[0], tag, bad) + unseal(g[1], tag, bad)) + (unseal(g[2], tag, bad) + unseal(g[3], tag, bad));
        if (__all((bad & TAG_MASK) == 0u)) break;
      }
      STAMP(4);
      LAS unsigned short* qd = (LAS unsigned short*)(L + L_QS) + g4 * 64 + c;
      const unsigned q01 = cvtpk(q[0], q[1]), q23 = cvtpk(q[2], q[3]);
      qd[0] = (unsigned short)q01; qd[16] = (unsigned short)(q01 >> 16); qd[32] = (unsigned short)q23; qd[48] = (unsigned short)(q23 >> 16);
      LDS_DRAIN();
      FL[FL_Q] = 1u;
    } else {
      const unsigned short bias = ((LAS unsigned short*)(L + L_XR + 1024 + 768))[((j * 18) & 7) + min(lane, 17)];
      const int f = j * 18 + lane, lc = f >> 6;
      const int cnt = lane < 18 ? (576 * lc + 575) / 162 - (32 * lc) / 9 + 1 : 0;
      const int ha = WS_H + lc * 5 * 256 + (f & 15) * 16;
      pace<SETTLE>(tpub);
      pace<TH>(t0);
      v4f hv;
#pragma nounroll
      for (int sp = 0; sp < SPIN_G; ++sp) {
        CBAR(); COUNT();
        v4u g[5];
#pragma unroll
        for (int k = 0; k < 5; ++k) g[k] = __builtin_amdgcn_raw_buffer_load_b128(rwh, k < cnt ? ha + k * 256 : OOB, 0, AUX_SC1);
        unsigned bad = 0;
        hv = v4f{0.f, 0.f, 0.f, 0.f};
#pragma unroll
        for (int k = 0; k < 5; ++k) hv += unseal(g[k], k < cnt ? tag : 0u, bad);
        if (__all((bad & TAG_MASK) == 0u)) break;
      }
      STAMP(4);
      const float hsum = (f & 32) ? ((f & 16) ? hv[3] : hv[2]) : ((f & 16) ? hv[1] : hv[0]);
      const float t = rbf(rbf(hsum) + asf((unsigned)bias << 16));
      const float z2 = 2.0f * 0.7978845608028654f * (t + 0.044715f * t * t * t);
      const float ge = t / (1.0f + __builtin_amdgcn_exp2f(-z2 * LOG2E));
      if (lane < 32) L[L_HS + lane] = lane < 18 ? cvtpk(ge, ge) : 0u;
      LDS_DRAIN();
      FL[FL_H] = 1u;
    }
    STAMP(5);
    VM_CLEAR();
  } else if (wave == W_C) {
#pragma unroll
    for (int w = 0; w < 4; ++w) *(LAS v4u*)(L + L_ORED + w * 256 + lane * 4) = v4u{EMPTY, EMPTY, EMPTY, EMPTY};
    LDS_DRAIN();
    const unsigned tag32 = __builtin_amdgcn_raw_buffer_load_b32(rws, 0, 0, AUX_SC1) + 1u, tag = tag32 & TAG_MASK;
    __builtin_amdgcn_s_barrier();
    lds_wait<1>(FL + FL_T0 + 1, 1u);
    unsigned t0 = __builtin_amdgcn_readfirstlane(FL[FL_T0]);
    if (SYNC) {
      if (s == 0 && lane < 16) __builtin_amdgcn_raw_buffer_store_b64(v2u{t0, tag32}, rws, WS_TT + lane * 8, h * 256, AUX_SC1);
      pace<TS>(t0);
      const v2u tt = __builtin_amdgcn_raw_buffer_load_b64(rws, WS_TT + c * 256, 0, AUX_SC1);
      const bool ok = tt.y == tag32;
      const float dsum = row_sum(ok ? (float)(int)(tt.x - t0) : 0.f), dcnt = row_sum(ok ? 1.f : 0.f);
      const float dm = dsum * __builtin_amdgcn_rcpf(fmaxf(dcnt, 1.f));
      t0 += __builtin_amdgcn_readfirstlane((int)fminf(fmaxf(dm, -150.f), 150.f));
    }
    *(volatile LAS v2u*)(FL + FL_T1) = v2u{t0, 1u};
    STAMP(1);
    lds_wait<4>(FL + FL_FW, 1u);
    const int hh = lane >> 5;
    v4f fw = *(LAS v4f*)(L + L_FW + hh * 4);
    int oaddr = WS_O + j * O_STRIDE + (hh * 128 + c * 8 + (g4 & 1) * 4) * 4;
    const v2u mlp = *(LAS v2u*)(L + L_G + (lane & 1) * 2);
    v4u mls = seal(v4f{asf(mlp.x), asf(mlp.y), asf(mlp.x), asf(mlp.y)}, tag);
    int maddr = lane < 4 ? WS_O + j * O_STRIDE + 1024 + lane * 16 : OOB;
    PIN(fw); PIN(oaddr); PIN(mls); PIN(maddr);
    pace<slot_time(44)>(t0);
    STAMP(2);
    v4f o;
#pragma nounroll
    for (int sp = 0; sp < SPIN_L; ++sp) {
      CBAR();
      v4u d[4];
#pragma unroll
      for (int w = 0; w < 4; ++w) d[w] = *(LAS v4u*)(L + L_ORED + w * 256 + lane * 4);
      o = __builtin_bit_cast(v4f, d[0]) * fw[0];
#pragma unroll
      for (int w = 1; w < 4; ++w) o += __builtin_bit_cast(v4f, d[w]) * fw[w];
      const float chk = (o[0] + o[1]) + (o[2] + o[3]);
      if (__all(chk == chk)) break;
      if (__all(full(d[0]) & full(d[1]) & full(d[2]) & full(d[3]))) break;
    }
    STAMP(3);
    st128(seal(o, tag), rws, oaddr, 0);
    st128(mls, rws, maddr, 0);
    STAMP(4);
    pace<TO>(now());
    FL[FL_OPUB] = 1u;
    VM_CLEAR();
  } else {
    const int aw = wave - W_A;
    if (aw == 3 && lane < 32) L[L_FLAG + lane] = 0u;
    LDS_DRAIN();
    __builtin_amdgcn_s_barrier();
    CBAR();
    if (TOUCH) {
      const int kvo = (h * SK + (SK - WIN) + s * 256) * 256;
      if (aw == 0) touch((const char*)kc + kvo);
      else {
        __builtin_amdgcn_s_sleep(TOUCH);
        if (aw == 1) { touch((const char*)vc + kvo); touch(out); }
        if (aw == 2) touch((const char*)wo + j * 18432);
        if (aw == 3) touch((const char*)w2 + j * 20736);
      }
    }
    const bool reducer = aw == 1 && (j & 31) < 9;
    const int hb = c >> 3;
    lds_wait<4>(FL + FL_PROG + aw, 2u);
    lds_wait<1>(FL + FL_PROG + aw, 3u);
    STAMP(1);
    v4u kf[16];
#pragma unroll
    for (int t = 0; t < 4; ++t)
#pragma unroll
      for (int kk = 0; kk < 4; ++kk)
        kf[t * 4 + kk] = *(LAS v4u*)(L + L_K + (aw * 16 + t * 4 + (c & 3)) * 256 + (c >> 2) * 64 + (((kk * 4 + g4) ^ c) * 4));
#pragma unroll
    for (int i = 0; i < 16; ++i) PIN(kf[i]);
    lds_wait<0>(FL + FL_Q, 1u);
    STAMP(2);
    v4u qf[4];
#pragma unroll
    for (int kk = 0; kk < 4; ++kk) qf[kk] = *(LAS v4u*)(L + L_QS + hb * 64 + (kk * 4 + g4) * 4);
    SB();
    v4f sc[4];
#pragma unroll
    for (int t = 0; t < 4; ++t) sc[t] = v4f{0.f, 0.f, 0.f, 0.f};
#pragma unroll
    for (int t = 0; t < 4; ++t)
#pragma unroll
      for (int kk = 0; kk < 4; ++kk) sc[t] = mfma(kf[t * 4 + kk], qf[kk], sc[t]);
    float sv[4];
#pragma unroll
    for (int e = 0; e < 4; ++e) {
      const float lo = (c & 1) ? sc[1][e] : sc[0][e], hi = (c & 1) ? sc[3][e] : sc[2][e];
      sv[e] = rbf(rbf((c & 2) ? hi : lo) * 0.08838834764831845f);
    }
    float m = fmaxf(fmaxf(sv[0], sv[1]), fmaxf(sv[2], sv[3]));
    m = fmaxf(m, dpp<XOR1>(m)); m = fmaxf(m, dpp<XOR2>(m));
    m = xrow_max(m);
    float p[4];
    v4u pk;
#pragma unroll
    for (int e = 0; e < 4; ++e) { p[e] = __builtin_amdgcn_exp2f((sv[e] - m) * LOG2E); pk[e] = cvtpk(p[e], p[e]); }
    *(LAS v4u*)(L + L_P + aw * 128 + (hb * 4 + g4) * 16 + (c & 3) * 4) = pk;
    float l = (p[0] + p[1]) + (p[2] + p[3]);
    l += dpp<XOR1>(l); l += dpp<XOR2>(l);
    l = xrow_sum(l);
    if ((lane & 55) == 0) *(LAS v2u*)(L + L_ML + aw * 4 + hb * 2) = v2u{asu(m), asu(l)};
    LDS_DRAIN();
    FL[FL_P + aw] = 1u;
    STAMP(3);
    const unsigned tag32 = L[L_XR + 512] + 1u, tag = tag32 & TAG_MASK;
    if (aw == 0) {
      lds_wait4<0>(FL + FL_P, 1u);
      const int hh = (lane >> 2) & 1, w = lane & 3;
      const v2u ml = *(LAS v2u*)(L + L_ML + w * 4 + hh * 2);
      float M = asf(ml.x);
      M = fmaxf(M, dpp<XOR1>(M)); M = fmaxf(M, dpp<XOR2>(M));
      const float fw = __builtin_amdgcn_exp2f((asf(ml.x) - M) * LOG2E);
      float Ls = asf(ml.y) * fw;
      Ls += dpp<XOR1>(Ls); Ls += dpp<XOR2>(Ls);
      if (lane < 8) L[L_FW + lane] = asu(fw);
      if ((lane & 59) == 0) *(LAS v2u*)(L + L_G + hh * 2) = v2u{asu(M), asu(Ls)};
      LDS_DRAIN();
      FL[FL_FW] = 1u;
      STAMP(4);
    } else if (reducer) {
      const int red = (j >> 5) * 9 + (j & 31);
      const int ocol = red * 8 + (lane & 1) * 4 + g4;
      const unsigned xb = ((LAS unsigned short*)(L + L_XR))[ocol];
      const unsigned bb = ((LAS unsigned short*)(L + L_XR + 768))[ocol & 7];
      float base = asf(xb << 16) + asf(bb << 16);
      PIN(base);
      lds_wait<4>(FL + FL_OPUB, 1u);
      lds_wait<0>(FL + FL_FPUB, 5u);
      pace<TF>(now());
      STAMP(4);
      v4f sum;
      bool done = false;
#pragma nounroll
      for (int sp = 0; sp < SPIN_G; ++sp) {
        CBAR(); COUNT();
        if (!done) {
          v4u pv[8];
#pragma unroll
          for (int i = 0; i < 8; ++i) pv[i] = __builtin_amdgcn_raw_buffer_load_b128(rws, (lane >> 1) * 64 + (lane & 1) * 16 + (i & 1) * 2048, WS_F + (red >> 1) * F_STRIDE + (red & 1) * 32 + (i >> 1) * 4096, AUX_SC1);
          unsigned bad = 0;
          sum = unseal(pv[0], tag, bad);
#pragma unroll
          for (int i = 1; i < 8; ++i) sum += unseal(pv[i], tag, bad);
          done = (bad & TAG_MASK) == 0u;
        }
        if (__all(done)) break;
      }
      STAMP(5);
      auto s02 = __builtin_amdgcn_permlane32_swap(asu(sum[0]), asu(sum[2]), false, false);
      auto s13 = __builtin_amdgcn_permlane32_swap(asu(sum[1]), asu(sum[3]), false, false);
      const float u0 = asf(s02[0]) + asf(s02[1]), u1 = asf(s13[0]) + asf(s13[1]);
      auto s01 = __builtin_amdgcn_permlane16_swap(asu(u0), asu(u1), false, false);
      float u = asf(s01[0]) + asf(s01[1]);
      u += dpp<ROR8>(u); u += dpp<ROR4>(u); u += dpp<XOR2>(u);
      u += base;
      if ((lane & 14) == 0) __builtin_amdgcn_raw_buffer_store_b16((unsigned short)cvtpk(u, u), RS(out, D * 2), ocol * 2, 0, 0);
      if (j == 0 && lane == 0) __builtin_amdgcn_raw_buffer_store_b32(tag32, rws, 0, 0, AUX_SC1);
      STAMP(6);
    }
    VM_CLEAR();
  }

}

extern "C" __device__ __attribute__((used, noinline)) void fused_decode_end() {}

extern "C" void kernel_launch(void* const* d_in, const int* in_sizes, int n_in, void* d_out, int out_size, void* d_ws, size_t ws_size,
                              hipStream_t stream) {
  fused_decode<<<256, NWAVE * 64, 0, stream>>>(d_in[3], d_in[1], d_in[6], d_in[2], d_in[4], d_in[8], d_ws, d_in[0], d_in[7], d_in[9], d_out);
}
```

```cpp
#include <hip/hip_runtime.h>
#include <stdint.h>
#include <type_traits>

typedef unsigned v4u __attribute__((ext_vector_type(4)));
typedef unsigned v2u __attribute__((ext_vector_type(2)));
typedef float v4f __attribute__((ext_vector_type(4)));
typedef float v2f __attribute__((ext_vector_type(2)));
typedef short v4s __attribute__((ext_vector_type(4)));
typedef __bf16 bf16x8 __attribute__((ext_vector_type(8)));
typedef __bf16 bf16x2 __attribute__((ext_vector_type(2)));
#define LAS __attribute__((address_space(3)))

constexpr int D = 576, SK = 16384, WIN = 4096, F = 4608;
constexpr int KV_BYTES = 16 * SK * 128 * 2;
constexpr int WQ_BYTES = D * 4096 * 2, W1_BYTES = D * F * 2;
constexpr int WS_TT = 4096  , WS_Q = 8192, WS_H = WS_Q + 256 * 256, WS_O = WS_H + 72 * 5 * 256, O_STRIDE = 1152,
              WS_F = WS_O + 256 * O_STRIDE, F_STRIDE = 256 * 64 + 256, WS_END = WS_F + 36 * F_STRIDE;
constexpr unsigned EMPTY = 0xFFFFFFFFu;
constexpr int OOB = 0x7FFFFFFF;
#define WINDOW 11
#define PACE10 152
#define PACE0 80
#define BLIND 3
#define AHEAD 6
#define LAG 3
#define LAG1 4
#define LAG2 7
#define LAG3 5
#define TQ (slot_time(27))
#define TH (slot_time(31))
#define TM (slot_time(48))
#define EARLY AHEAD
#define DRIFT0 (SMP + 19)
#define TS (slot_time(SMP + 13))
#define SYNC 1
#define OATOM 0
#define TOUCH 30
#define EXTRA 0
#define STAG 0
#define PV0 42
#define TG 15
#define F4S 43
#define F4D 1
#define FCOST 92
#define SMP 12
#define RTT0 55
#define HAT 47
#define WE 11
#define WEN 28
#define NQ 27
#define NH 33
#define TA (slot_time(9))
#define GAP 200
#define KVORD 1
#define CAP 35
#define OG 49
#define SETTLE 60
#define TO 35
#define TF 25
constexpr int AUX_NT = 2, AUX_SC1 = 16, W = WINDOW;
#define ABL 0
constexpr int SPIN_G = ABL ? 1 : 20000, SPIN_L = 400000;
constexpr float LOG2E = 1.4426950408889634f;

constexpr int L_WQ = 0  , L_K = 5120  , L_W1 = L_K + 16384  , L_FLAG = L_W1 + 6144  ;
constexpr int FL_Q = 0, FL_H = 1, FL_PROG = 4  , FL_P = 8  , FL_KDONE = 13, FL_FW = 14, FL_G = 15, FL_VDONE = 16, FL_OPUB = 17,
              FL_FPUB = 18, FL_T0 = 20  , FL_T1 = 22  , FL_TS = 24  , FL_SLOT = 26;
constexpr int L_XR = L_FLAG + 32  , L_Y = L_XR + 2 * 1024  , L_QS = L_Y + 2 * 512  , L_HS = L_QS + 128  ,
              L_ML = L_HS + 32  , L_FW = L_ML + 16  , L_G = L_FW + 16  , L_OS = L_G + 16  , L_ORED = L_OS + 16  ,
              L_P = L_ORED + 1280  , L_END = L_P + 512;
constexpr int YZ = 192;

#define RS(p, n) __builtin_amdgcn_make_buffer_rsrc((void*)(p), 0, (int)(n), 0x00020000)
#define SB() __builtin_amdgcn_sched_barrier(0)
#define CBAR() asm volatile("" ::: "memory")
#define LDS_DRAIN() asm volatile("s_waitcnt lgkmcnt(0)" ::: "memory")
#define VM_DRAIN() asm volatile("s_waitcnt vmcnt(0)" ::: "memory")
#define VM_CLEAR() __builtin_amdgcn_s_waitcnt(0x0F70)
#define PIN(x) asm volatile("" : "+v"(x))

#define COUNT() do {} while (0)
#define STAMP(i) do {} while (0)

__device__ __forceinline__ float asf(unsigned u) { return __builtin_bit_cast(float, u); }
__device__ __forceinline__ unsigned asu(float f) { return __builtin_bit_cast(unsigned, f); }
__device__ __forceinline__ unsigned cvtpk(float lo, float hi) {
  return __builtin_bit_cast(unsigned, __builtin_convertvector(v2f{lo, hi}, bf16x2));
}
__device__ __forceinline__ v2f unpk(unsigned p) { return v2f{asf(p << 16), asf(p & 0xFFFF0000u)}; }
__device__ __forceinline__ float rbf(float f) { return asf(cvtpk(0.f, f)); }
__device__ __forceinline__ float vmax(float a, float b) { float r; asm("v_max_f32 %0, %1, %2" : "=v"(r) : "v"(a), "v"(b)); return r; }
__device__ __forceinline__ float vmax3(float a, float b, float c) { float r; asm("v_max3_f32 %0, %1, %2, %3" : "=v"(r) : "v"(a), "v"(b), "v"(c)); return r; }
template <int CTRL> __device__ __forceinline__ float dpp(float x) {
  return asf(__builtin_amdgcn_mov_dpp(asu(x), CTRL, 0xf, 0xf, true));
}
constexpr int XOR1 = 0xB1, XOR2 = 0x4E, XOR7 = 0x141, ROR8 = 0x128, ROR4 = 0x124;
__device__ __forceinline__ float xrow_sum(float x) {
  auto s = __builtin_amdgcn_permlane16_swap(asu(x), asu(x), false, false);
  x = asf(s[0]) + asf(s[1]);
  auto t = __builtin_amdgcn_permlane32_swap(asu(x), asu(x), false, false);
  return asf(t[0]) + asf(t[1]);
}
__device__ __forceinline__ float xrow_max(float x) {
  auto s = __builtin_amdgcn_permlane16_swap(asu(x), asu(x), false, false);
  x = fmaxf(asf(s[0]), asf(s[1]));
  auto t = __builtin_amdgcn_permlane32_swap(asu(x), asu(x), false, false);
  return fmaxf(asf(t[0]), asf(t[1]));
}
__device__ __forceinline__ float row_sum(float x) {
  x += dpp<XOR1>(x); x += dpp<XOR2>(x); x += dpp<XOR7>(x); x += dpp<ROR8>(x);
  return x;
}
__device__ __forceinline__ float row_max(float x) {
  asm("s_nop 1\n\tv_max_f32_dpp %0, %0, %0 quad_perm:[1,0,3,2] row_mask:0xf bank_mask:0xf\n\ts_nop 1\n\t"
      "v_max_f32_dpp %0, %0, %0 quad_perm:[2,3,0,1] row_mask:0xf bank_mask:0xf\n\ts_nop 1\n\t"
      "v_max_f32_dpp %0, %0, %0 row_half_mirror row_mask:0xf bank_mask:0xf\n\ts_nop 1\n\t"
      "v_max_f32_dpp %0, %0, %0 row_ror:8 row_mask:0xf bank_mask:0xf" : "+v"(x));
  return x;
}
__device__ __forceinline__ v4f mfma(v4u a, v4u b, v4f c) {
  return __builtin_amdgcn_mfma_f32_16x16x32_bf16(__builtin_bit_cast(bf16x8, a), __builtin_bit_cast(bf16x8, b), c, 0, 0, 0);
}
__device__ __forceinline__ v4u box(v4f v) {
  v4u r;
#pragma unroll
  for (int i = 0; i < 4; ++i) r[i] = min(asu(v[i]), 0xFFFFFFFEu);
  return r;
}
constexpr unsigned TAG_MASK = 0x3FFu;
__device__ __forceinline__ v4u seal(v4f v, unsigned tag) {
  v4u r;
#pragma unroll
  for (int i = 0; i < 4; ++i) r[i] = (asu(v[i]) & ~TAG_MASK) | tag;
  return r;
}
__device__ __forceinline__ v4f unseal(v4u w, unsigned tag, unsigned& bad) {
  const v4u x = w ^ tag;
  bad |= (x.x | x.y) | (x.z | x.w);
  return __builtin_bit_cast(v4f, x);
}
__device__ __forceinline__ bool full(v4u v) { return max(max(v.x, v.y), max(v.z, v.w)) != EMPTY; }
template <int NAP = 2> __device__ __forceinline__ void lds_wait(volatile LAS unsigned* f, unsigned need) {
#pragma nounroll
  for (int sp = 0; sp < SPIN_L; ++sp) {
    if ((unsigned)__builtin_amdgcn_readfirstlane(*f) >= need) break;
    if (NAP) __builtin_amdgcn_s_sleep(NAP);
  }
  CBAR();
}
template <int NAP> __device__ __forceinline__ void lds_wait4(volatile LAS unsigned* f, unsigned need) {
#pragma nounroll
  for (int sp = 0; sp < SPIN_L; ++sp) {
    const v4u v = *(volatile LAS v4u*)f;
    if ((unsigned)__builtin_amdgcn_readfirstlane(min(min(v.x, v.y), min(v.z, v.w))) >= need) break;
    if (NAP) __builtin_amdgcn_s_sleep(NAP);
  }
  CBAR();
}
__device__ __forceinline__ void lds_inc(LAS unsigned* f, int lane) {
  if (lane == 0) __hip_atomic_fetch_add(f, 1u, __ATOMIC_RELAXED, __HIP_MEMORY_SCOPE_WORKGROUP);
}
typedef __attribute__((address_space(1))) unsigned long long gu64;
__device__ __forceinline__ v2u peek(void* ws, int off) {
  unsigned long long zero = 0ull;
  asm volatile("" : "+v"(zero));
  const unsigned long long r = __hip_atomic_fetch_or((gu64*)((char*)ws + off), zero, __ATOMIC_RELAXED, __HIP_MEMORY_SCOPE_AGENT);
  return v2u{(unsigned)r, (unsigned)(r >> 32)};
}
__device__ __forceinline__ void touch_and_barrier(const void* p, unsigned o0, unsigned o1, unsigned o2, const void* q, unsigned o3) {
  unsigned d0, d1, d2, d3;
  asm volatile("s_load_dword %0, %4, %5\n\ts_load_dword %1, %4, %6\n\ts_load_dword %2, %4, %7\n\ts_load_dword %3, %8, %9\n\ts_barrier\n\ts_waitcnt lgkmcnt(0)"
               : "=&s"(d0), "=&s"(d1), "=&s"(d2), "=&s"(d3) : "s"(p), "s"(o0), "s"(o1), "s"(o2), "s"(q), "s"(o3) : "memory");
}
__device__ __forceinline__ void touch2(const void* p, unsigned o0, unsigned o1) {
  unsigned d0, d1;
  asm volatile("s_load_dword %0, %2, %3\n\ts_load_dword %1, %2, %4\n\ts_waitcnt lgkmcnt(0)" : "=&s"(d0), "=&s"(d1) : "s"(p), "s"(o0), "s"(o1) : "memory");
}
__device__ __forceinline__ unsigned now() {
  unsigned long long t;
  asm volatile("s_memrealtime %0\n\ts_waitcnt lgkmcnt(0)" : "=s"(t)::"memory");
  return (unsigned)t;
}
template <int T> __device__ __forceinline__ void pace(unsigned t0) {
  asm volatile("1:\n\ts_memrealtime s[90:91]\n\ts_waitcnt lgkmcnt(0)\n\ts_sub_u32 s90, s90, %0\n\ts_cmpk_lt_u32 s90, %1\n\ts_cbranch_scc1 1b"
               ::"s"(__builtin_amdgcn_readfirstlane(t0)), "i"(T) : "memory", "scc", "s90", "s91");
}
constexpr int slot_cost(int n) { return n == 10 ? 30 : n == 42 ? GAP : (n >= 43 ? (n == 47 ? 60 : FCOST) : 100); }
constexpr int slot_time(int n) {
  if (n < AHEAD) return (n - BLIND + 1) * 15;
  int c = 0;
  for (int i = AHEAD; i < n; ++i) c += slot_cost(i);
  return PACE0 + c * PACE10 / 1000;
}
constexpr int pv_done(int n) { return n < PV0 ? 0 : ((n - PV0) * 5 + 2 < n - 26 - LAG ? (n - PV0) * 5 + 2 : n - 26 - LAG); }
template <int N> __device__ __forceinline__ void vmwait() { asm volatile("s_waitcnt vmcnt(%0)" ::"i"(N) : "memory"); }
__device__ __forceinline__ void st128(v4u d, __amdgpu_buffer_rsrc_t r, int voff, int soff) {
  __builtin_amdgcn_raw_buffer_store_b128(d, r, voff, soff, AUX_SC1);
  asm volatile("s_nop 1" ::"v"(d) : "memory");
}
__device__ __forceinline__ void dma(__amdgpu_buffer_rsrc_t r, int voff, int soff, int ldsaddr) {
  asm volatile("s_mov_b32 m0, %0\n\ts_nop 0\n\tbuffer_load_dwordx4 %1, %2, %3 offen nt lds" ::"s"(ldsaddr), "v"(voff), "s"(r), "s"(soff) : "memory");
}
__device__ __forceinline__ void dma_dev(__amdgpu_buffer_rsrc_t r, int voff, int ldsaddr) {
  asm volatile("s_mov_b32 m0, %0\n\ts_nop 0\n\tbuffer_load_dwordx4 %1, %2, 0 offen sc1 lds" ::"s"(ldsaddr), "v"(voff), "s"(r) : "memory");
}
__device__ __forceinline__ v4u keep_mask(int m) {
  v4u k;
#pragma unroll
  for (int v = 0; v < 4; ++v) k[v] = ((m >> 1) == v) ? ((m & 1) ? 0xFFFF0000u : 0x0000FFFFu) : 0u;
  return k;
}
template <int I, int N, class Fn> __device__ __forceinline__ void static_for(Fn&& fn) {
  if constexpr (I < N) { fn(std::integral_constant<int, I>{}); static_for<I + 1, N>(fn); }
}
__device__ __forceinline__ void layernorm(unsigned (&p)[5], int lane) {
  const float c0 = asf(__builtin_amdgcn_readfirstlane(p[0]) << 16);
  v2f v[5], d[5];
#pragma unroll
  for (int i = 0; i < 5; ++i) { v[i] = unpk(p[i]); d[i] = v[i] - c0; }
  if (lane >= 32) d[4] = v2f{0.f, 0.f};
  const v2f s1v = (d[0] + d[1]) + (d[2] + d[3]) + d[4];
  v2f s2v = d[0] * d[0];
#pragma unroll
  for (int i = 1; i < 5; ++i) s2v = __builtin_elementwise_fma(d[i], d[i], s2v);
  const float s1 = xrow_sum(row_sum(s1v.x + s1v.y)) * (1.0f / D), s2 = xrow_sum(row_sum(s2v.x + s2v.y)) * (1.0f / D);
  const float var = fmaxf(fmaf(-s1, s1, s2), 0.f);
  const float mb = rbf(c0 + s1), sd = rbf(__builtin_amdgcn_sqrtf(rbf(rbf(var) + 1e-5f)));
  const float inv = __builtin_amdgcn_rcpf(sd);
#pragma unroll
  for (int i = 0; i < 5; ++i) {
    const v2f t = v[i] - mb;
    const v2f u = v2f{rbf(t.x), rbf(t.y)} * inv;
    p[i] = cvtpk(u.x, u.y);
  }
}

constexpr int W_F4 = 4, W_C = 5, W_P = 6, W_A = 8, NWAVE = 12;

extern "C" __global__ void __launch_bounds__(NWAVE * 64, 3)
fused_decode(const void* __restrict__ wq, const void* __restrict__ kc, const void* __restrict__ w1, const void* __restrict__ vc,
             const void* __restrict__ wo, const void* __restrict__ w2, void* __restrict__ ws, const void* __restrict__ x,
             const void* __restrict__ b1, const void* __restrict__ b2, void* __restrict__ out) {
  __shared__ __attribute__((aligned(16))) unsigned smem[L_END];
  LAS unsigned* L = (LAS unsigned*)smem;
  volatile LAS unsigned* FL = (volatile LAS unsigned*)smem + L_FLAG;
  const int tid = threadIdx.x, lane = tid & 63, wave = __builtin_amdgcn_readfirstlane(tid >> 6);
  const int b = blockIdx.x, j = (b & 7) * 32 + (b >> 3), h = j >> 4, s = j & 15;
  const int r8 = lane >> 3, c8 = lane & 7, g4 = lane >> 4, c = lane & 15;
  const int lcA = (j * 9) >> 5, rowA = 18 * ((j * 9) & 31);
  const int lds0 = (int)(unsigned)(uintptr_t)L;
  if (!(ABL & 64) && (wave >> 1) == (W_P >> 1)) {
    const int dst = lds0 + (L_XR + (wave & 1) * 1024) * 4;
    dma_dev(RS(x, D * 2), lane * 16, dst);
    dma_dev(RS(x, D * 2), lane * 16 + 1024, dst + 1024);
    dma_dev(RS(ws, 16), lane * 16, dst + 2048);
    const int red8 = ((j >> 5) * 9 + min(j & 31, 8)) * 16;
    dma_dev(RS((wave & 1) ? (const char*)b1 : (const char*)b2, (wave & 1) ? F * 2 : D * 2), lane < 4 ? lane * 16 + ((wave & 1) ? (j * 36) & ~15 : red8) : OOB, dst + 3072);
  }
  if (!(ABL & 128) && wave == W_C) {
    unsigned long long pc;
    unsigned pcoff, len;
    asm volatile("s_getpc_b64 %0\n.Lpc%=:\n\ts_mov_b32 %1, .Lpc%=-fused_decode\n\ts_mov_b32 %2, fused_decode_end-fused_decode"
                 : "=s"(pc), "=s"(pcoff), "=s"(len));
    dma_dev(RS(pc - pcoff, len), lane < 40 ? lane * 16 + (b >> 3) * 640 : OOB, lds0 + (L_ORED + 1024) * 4);
  }
  const unsigned kvo = (h * SK + (SK - WIN) + s * 16) * 256;
  if (wave == W_A) {
    const unsigned c1 = lcA * 128;
    touch_and_barrier(w1, rowA * 9216 + c1, min(rowA + 161, D - 1) * 9216 + c1, c1, kc, kvo);
  } else if (wave >= 4) {
    if (wave == W_F4 && lane < 32) L[L_FLAG + lane] = 0u;
    LDS_DRAIN();
    __builtin_amdgcn_s_barrier();
    CBAR();
  }
  const auto rws = RS(ws, WS_END);
  const auto rwq = RS(ws, (ABL & 4) ? 0 : WS_END), rwh = RS(ws, (ABL & 8) ? 0 : WS_END), rwm = RS(ws, (ABL & 16) ? 0 : WS_END), rwp = RS(ws, (ABL & 32) ? 0 : WS_END);

  if (__builtin_expect(wave <= W_F4, 1)) {
    v4u wf[9], ha[5], mskf, mw, ow;
    int vo, v2, faddr, oga, mga;
    unsigned tag;
    auto prep_addr = [&]() {
      const bool live = wave < 4 || c < 8;
      vo = live ? g4 * 4608 + wave * 256 + c * 16 : OOB - 4096;
      v2 = live ? g4 * 1152 + wave * 256 + c * 16 : OOB - 5 * 4608;
      faddr = lane < 32 && live ? WS_F + (wave * 8 + (c >> 1)) * F_STRIDE + j * 64 + (c & 1) * 32 + g4 * 16 : OOB;
      oga = WS_O + (h * 16 + c) * O_STRIDE + s * 64 + g4 * 16;
      mga = WS_O + 1024 + (h * 16 + c) * O_STRIDE + (s >> 3) * 16;
      mskf = keep_mask(c);
      PIN(mga); PIN(vo); PIN(v2); PIN(faddr); PIN(oga); PIN(mskf);
    };
    auto prep_h = [&]() {
      lds_wait<0>(FL + FL_H, 1u);
      unsigned hv[5];
      const unsigned ep = L[L_XR + 512];
#pragma unroll
      for (int rg = 0; rg < 5; ++rg) hv[rg] = L[L_HS + rg * 4 + g4];
      SB();
      tag = (ep + 1u) & TAG_MASK;
#pragma unroll
      for (int rg = 0; rg < 5; ++rg) ha[rg] = mskf & hv[rg];
      asm volatile("" : "+v"(tag), "+v"(ha[0]), "+v"(ha[1]), "+v"(ha[2]), "+v"(ha[3]), "+v"(ha[4]));
    };
    if (__builtin_expect(wave < 4, 1)) {
      const auto rq = RS(wq, WQ_BYTES), rk = RS(kc, KV_BYTES), rv = RS(vc, KV_BYTES), r1 = RS(w1, W1_BYTES);
      const int qs = (s >> 2) * 144 * 8192 + h * 512 + (s & 3) * 128;
      const int kvb = (h * SK + (SK - WIN) + s * 16) * 256 + wave * 1024, rot = h;
      const int cs = (c8 ^ (r8 & 6)) * 16;
      __amdgpu_buffer_rsrc_t ro, r2;
      v4u vv[16], pk[4], mskv, alast;
      v4f acc = {0.f, 0.f, 0.f, 0.f};
      unsigned t0;
      int drift = 0;
      static_for<0, 52>([&](auto nc) {
        constexpr int n = nc;
        if (n == 1) t0 = now();
        if (n == BLIND) __builtin_amdgcn_s_barrier();
        if (n == AHEAD) {
          vmwait<AHEAD - 1>();
          const unsigned t1 = now() - slot_time(n);
          if ((int)(t1 - t0) > 0) t0 = t1 + min((int)(t1 - t0), EXTRA);
          if (wave == 0) *(volatile LAS v2u*)(FL + FL_T0) = v2u{t0, 1u};
        }
        if (n < WEN) { if (n >= WE) vmwait<WE - 1>(); } else vmwait<W - 1>();
        if constexpr (n >= 43) pace<slot_time(n)>(t0 + drift);
        else if constexpr (n >= DRIFT0) pace<slot_time(n)>(t0 + ((drift * ((n - DRIFT0 + 1) * 256 / (43 - DRIFT0))) >> 8));
        else if constexpr (n >= BLIND) pace<slot_time(n)>(t0);
        if constexpr (n < 5) {
          dma(rq, min((n * 4 + wave) * 8 + r8, 143) * 8192 + cs, qs, lds0 + L_WQ * 4 + (n * 4 + wave) * 1024);
        } else if constexpr (n < 11) {
          constexpr int i = n - 5;
          const unsigned v = (unsigned)((rowA + min((i * 4 + wave) * 8 + r8, 161)) * 9216 + cs);
          dma(r1, (int)min(v, v - (unsigned)(W1_BYTES - 128)), lcA * 128, lds0 + L_W1 * 4 + (i * 4 + wave) * 1024);
        } else if constexpr (n < 27) {
          constexpr int i = n - 11;
          dma(rk, g4 * 256 + ((c ^ (g4 * 4 + (i & 3))) * 16), kvb + (((KVORD ? (i & 3) * 4 + (i >> 2) : i) + rot) & 15) * 65536, lds0 + L_K * 4 + (wave * 16 + i) * 1024);
        } else if constexpr (n < 43) {
          constexpr int i = n - 27;
          vv[i] = __builtin_amdgcn_raw_buffer_load_b128(rv, lane * 16, kvb + (((KVORD ? (i & 3) * 4 + (i >> 2) : i) + rot) & 15) * 65536, AUX_NT);
        } else if constexpr (n < 48) {
          wf[n - 43] = __builtin_amdgcn_raw_buffer_load_b128(r2, v2 + (n - 43) * 4608, 0, AUX_NT);
        } else {
          wf[n - 43] = __builtin_amdgcn_raw_buffer_load_b128(ro, vo, (n - 48) * 1152, AUX_NT);
        }
        SB();
        if (n == AHEAD) STAMP(7);
        if (n == 4 + LAG1) { vmwait<LAG1>(); FL[FL_PROG + wave] = 1u; STAMP(1); }
        if (n == 10 + LAG2) { vmwait<LAG2>(); FL[FL_PROG + wave] = 2u; }
        if (n == 26 + LAG3) { vmwait<LAG3>(); FL[FL_PROG + wave] = 3u; STAMP(2); }
        if (n == SMP + 3 && wave == 0) {
          if (s == 0) {
            vmwait<3>();
            const unsigned t1 = now() - (slot_time(SMP) + RTT0);
            *(volatile LAS v2u*)(FL + FL_TS) = v2u{(int)(t1 - t0) > 0 ? t1 : t0, 1u};
          }
        }
        if (n == 21) prep_addr();
        if (n == 22) {
          mskv = keep_mask(c8);
          PIN(mskv);
          ro = RS((const char*)wo + j * 18432, 18432);
          r2 = RS((const char*)w2 + j * 20736, 20736);
          asm volatile("" : "+s"(ro), "+s"(r2));
        }
        if ((n == NQ || n == NH) && wave == 0) FL[FL_SLOT] = n;
        if (n == DRIFT0 - 1 || n == 36) {
          const v2u t1 = *(volatile LAS v2u*)(FL + FL_T1);
          drift = __builtin_amdgcn_readfirstlane(t1.y) ? (int)(__builtin_amdgcn_readfirstlane(t1.x) - t0) : 0;
        }
        if (n == PV0) {
          lds_wait<0>(FL + FL_P + wave, 1u);
          STAMP(3);
#pragma unroll
          for (int t = 0; t < 4; ++t) pk[t] = *(LAS v4u*)(L + L_P + wave * 128 + ((c >> 3) * 4 + g4) * 16 + t * 4);
          alast = mskv & pk[3][3];
          PIN(alast);
        }
        if constexpr (n >= PV0 && n <= 42 + LAG) {
          constexpr int done = pv_done(n - 1), ready = pv_done(n);
#pragma unroll
          for (int i = done; i < ready; ++i) acc = mfma(i == 15 ? alast : mskv & pk[i >> 2][i & 3], vv[i], acc);
          if (n == 42 + LAG) {
            *(LAS v4u*)(L + L_ORED + wave * 256 + lane * 4) = box(acc);
            STAMP(4);
          }
        }
        if (n == HAT) prep_h();
        if (n == OG) {
          lds_wait<0>(FL + FL_OPUB, 1u);
          STAMP(5);
          mw = __builtin_amdgcn_raw_buffer_load_b128(rws, mga, 0, AUX_SC1);
          ow = __builtin_amdgcn_raw_buffer_load_b128(rws, oga, 0, AUX_SC1);
        }
        SB();
      });
    } else {
      prep_addr();
      const auto ro = RS((const char*)wo + j * 18432, 18432), r2 = RS((const char*)w2 + j * 20736, 20736);
      lds_wait<4>(FL + FL_T1 + 1, 2u);
      const unsigned t0 = __builtin_amdgcn_readfirstlane(FL[FL_T1]);
      static_for<0, 9>([&](auto nc) {
        constexpr int n = nc;
        pace<slot_time(F4S + n * F4D)>(t0);
        if constexpr (n < 5) asm volatile("buffer_load_dwordx4 %0, %1, %2, 0 offen nt" : "=v"(wf[n]) : "v"(v2 + n * 4608), "s"(r2) : "memory");
        else asm volatile("buffer_load_dwordx4 %0, %1, %2, 0 offen offset:%3 nt" : "=v"(wf[n]) : "v"(vo), "s"(ro), "i"((n - 5) * 1152) : "memory");
        SB();
        if (n + 43 == HAT) prep_h();
        if (n + 43 == OG) {
          lds_wait<0>(FL + FL_OPUB, 1u);
          STAMP(5);
          asm volatile("buffer_load_dwordx4 %0, %2, %4, 0 offen sc1\n\tbuffer_load_dwordx4 %1, %3, %4, 0 offen sc1"
                       : "=&v"(mw), "=&v"(ow) : "v"(mga), "v"(oga), "s"(rws) : "memory");
        }
        SB();
      });
    }
    v4f a2 = {0.f, 0.f, 0.f, 0.f};
#pragma unroll
    for (int rg = 0; rg < 5; ++rg) a2 = mfma(ha[rg], wf[rg], a2);
    PIN(a2);
    SB();
    unsigned bad = 0;
    v4f ml = unseal(mw, tag, bad), u = unseal(ow, tag, bad);
    COUNT();
    if (__builtin_expect(!__all((bad & TAG_MASK) == 0u), 0)) {
#pragma nounroll
      for (int sp = 0; sp < SPIN_G; ++sp) {
        COUNT();
        CBAR();
        bad = 0;
        ml = unseal(__builtin_amdgcn_raw_buffer_load_b128(rws, mga, 0, AUX_SC1), tag, bad);
        u = unseal(__builtin_amdgcn_raw_buffer_load_b128(rws, oga, 0, AUX_SC1), tag, bad);
        if (__all((bad & TAG_MASK) == 0u)) break;
      }
    }
    STAMP(6);
    const float gw = __builtin_amdgcn_exp2f((ml[0] - row_max(ml[0])) * LOG2E);
    float r[5] = {u[0] * gw, u[1] * gw, u[2] * gw, u[3] * gw, ml[1] * gw};
#pragma unroll
    for (int i = 4; i >= 0; --i) r[i] += dpp<XOR1>(r[i]);
#pragma unroll
    for (int i = 4; i >= 0; --i) r[i] += dpp<XOR2>(r[i]);
#pragma unroll
    for (int i = 4; i >= 0; --i) r[i] += dpp<XOR7>(r[i]);
#pragma unroll
    for (int i = 4; i >= 0; --i) r[i] += dpp<ROR8>(r[i]);
    const float inv = __builtin_amdgcn_rcpf(r[4]);
    v4u oa[4];
#pragma unroll
    for (int rg = 0; rg < 4; ++rg) oa[rg] = mskf & cvtpk(r[rg] * inv, r[rg] * inv);
    SB();
#pragma unroll
    for (int rg = 0; rg < 4; ++rg) a2 = mfma(oa[rg], wf[5 + rg], a2);
    st128(seal(a2, tag), rws, faddr, 0);
    lds_inc(L + L_FLAG + FL_FPUB, lane);
    if (wave != 3) STAMP(7);
  } else if ((wave >> 1) == (W_P >> 1)) {
    const int pw = wave - W_P;
    LAS unsigned* Y = L + L_Y + pw * 512;
    *(LAS v4u*)(Y + lane * 8) = v4u{0u, 0u, 0u, 0u};
    *(LAS v4u*)(Y + lane * 8 + 4) = v4u{0u, 0u, 0u, 0u};
    const int nA = min(162, D - rowA), sh = pw == 0 ? 0 : (rowA & 2);
    const int ya = YZ + sh + (pw == 0 ? (s >> 2) * 144 : rowA), yb = pw == 0 ? ya : (nA == 162 ? 0 : YZ + sh - nA);
    const LAS char* ap = (const LAS char*)Y + ((c < 8 ? ya : yb) + g4 * 4) * 2;
    const LAS char* bp = (const LAS char*)(L + (pw == 0 ? L_WQ : L_W1)) + (g4 * 4 + (c >> 2)) * 128 + (c & 3) * 8;
    const int key = (g4 & 1) * 2 + (c >> 3);
    const int gA = pw == 0 ? WS_Q + j * 256 : WS_H + (lcA * 5 + j - (32 * lcA) / 9) * 256;
    int gout = g4 == 0 ? gA + c * 16 : (g4 == 2 && pw == 1 && nA < 162 ? WS_H + (lcA + 1) * 5 * 256 + c * 16 : OOB);
    PIN(gout);
    VM_DRAIN();
    STAMP(1);
    unsigned xp[5];
#pragma unroll
    for (int i = 0; i < 5; ++i) xp[i] = L[L_XR + pw * 1024 + i * 64 + lane];
    const unsigned tag = (L[L_XR + pw * 1024 + 512] + 1u) & TAG_MASK;
#pragma nounroll
    for (int rep = 0; rep <= pw; ++rep) layernorm(xp, lane);
#pragma unroll
    for (int i = 0; i < 5; ++i)
      if (i < 4 || lane < 32) Y[(YZ + sh) / 2 + i * 64 + lane] = xp[i];
    v4u af[6];
#pragma unroll
    for (int ks = 0; ks < 6; ++ks) {
      const v2u lo = *(const LAS v2u*)(ap + ks * 64), hi = *(const LAS v2u*)(ap + ks * 64 + 32);
      af[ks] = v4u{lo.x, lo.y, hi.x, hi.y};
    }
    if (pw == 0) { af[4].z = 0u; af[4].w = 0u; }
    af[5] = v4u{g4 == 0 ? af[5].x : 0u, 0u, 0u, 0u};
#pragma unroll
    for (int ks = 0; ks < 6; ++ks) PIN(af[ks]);
    STAMP(2);
    lds_wait4<1>(FL + FL_PROG, pw + 1u);
    v4f acc[4];
    {
      v4u bf[5][4];
#pragma unroll
      for (int ks = 0; ks < 5; ++ks)
#pragma unroll
        for (int cg = 0; cg < 4; ++cg) {
          const LAS char* q = bp + ((cg ^ key) * 32) + ks * 4096;
          const v2u lo = __builtin_bit_cast(v2u, __builtin_amdgcn_ds_read_tr16_b64_v4i16((LAS v4s*)q));
          const v2u hi = __builtin_bit_cast(v2u, __builtin_amdgcn_ds_read_tr16_b64_v4i16((LAS v4s*)(q + 2048)));
          bf[ks][cg] = v4u{lo.x, lo.y, hi.x, hi.y};
        }
      SB();
#pragma unroll
      for (int cg = 0; cg < 4; ++cg) acc[cg] = v4f{0.f, 0.f, 0.f, 0.f};
#pragma unroll
      for (int ks = 0; ks < 5; ++ks)
#pragma unroll
        for (int cg = 0; cg < 4; ++cg) acc[cg] = mfma(af[ks], bf[ks][cg], acc[cg]);
    }
    if (pw == 1) {
#pragma unroll
      for (int cg = 0; cg < 4; ++cg) {
        const v2u lo = __builtin_bit_cast(v2u, __builtin_amdgcn_ds_read_tr16_b64_v4i16((LAS v4s*)(bp + ((cg ^ key) * 32) + 5 * 4096)));
        acc[cg] = mfma(af[5], v4u{lo.x, lo.y, 0u, 0u}, acc[cg]);
      }
    }
    for (int i = 0; i < STAG * s; ++i) __builtin_amdgcn_s_sleep(1);
    st128(seal(v4f{acc[0][0], acc[1][0], acc[2][0], acc[3][0]}, tag), rwp, gout, 0);
    STAMP(3);
    const unsigned tpub = now();
    if (pw == 0) {
      pace<SETTLE>(tpub);
      lds_wait<2>(FL + FL_SLOT, NQ);
      v4f q;
#pragma nounroll
      for (int sp = 0; sp < SPIN_G; ++sp) {
        CBAR(); COUNT();
        v4u g[4];
#pragma unroll
        for (int i = 0; i < 4; ++i) g[i] = __builtin_amdgcn_raw_buffer_load_b128(rwq, lane * 16 + i * 1024, WS_Q + h * 4096, AUX_SC1);
        unsigned bad = 0;
        q = (unseal(g[0], tag, bad) + unseal(g[1], tag, bad)) + (unseal(g[2], tag, bad) + unseal(g[3], tag, bad));
        if (__all((bad & TAG_MASK) == 0u)) break;
      }
      STAMP(4);
      LAS unsigned short* qd = (LAS unsigned short*)(L + L_QS) + g4 * 64 + c;
      const unsigned q01 = cvtpk(q[0], q[1]), q23 = cvtpk(q[2], q[3]);
      qd[0] = (unsigned short)q01; qd[16] = (unsigned short)(q01 >> 16); qd[32] = (unsigned short)q23; qd[48] = (unsigned short)(q23 >> 16);
      LDS_DRAIN();
      FL[FL_Q] = 1u;
    } else {
      const unsigned short bias = ((LAS unsigned short*)(L + L_XR + 1024 + 768))[((j * 18) & 7) + min(lane, 17)];
      const int f = j * 18 + lane, lc = f >> 6;
      const int cnt = lane < 18 ? (576 * lc + 575) / 162 - (32 * lc) / 9 + 1 : 0;
      const int ha = WS_H + lc * 5 * 256 + (f & 15) * 16;
      pace<SETTLE>(tpub);
      lds_wait<2>(FL + FL_SLOT, NH);
      v4f hv;
#pragma nounroll
      for (int sp = 0; sp < SPIN_G; ++sp) {
        CBAR(); COUNT();
        v4u g[5];
#pragma unroll
        for (int k = 0; k < 5; ++k) g[k] = __builtin_amdgcn_raw_buffer_load_b128(rwh, k < cnt ? ha + k * 256 : OOB, 0, AUX_SC1);
        unsigned bad = 0;
        hv = v4f{0.f, 0.f, 0.f, 0.f};
#pragma unroll
        for (int k = 0; k < 5; ++k) hv += unseal(g[k], k < cnt ? tag : 0u, bad);
        if (__all((bad & TAG_MASK) == 0u)) break;
      }
      STAMP(4);
      const float hsum = (f & 32) ? ((f & 16) ? hv[3] : hv[2]) : ((f & 16) ? hv[1] : hv[0]);
      const float t = rbf(rbf(hsum) + asf((unsigned)bias << 16));
      const float z2 = 2.0f * 0.7978845608028654f * (t + 0.044715f * t * t * t);
      const float ge = t / (1.0f + __builtin_amdgcn_exp2f(-z2 * LOG2E));
      if (lane < 32) L[L_HS + lane] = lane < 18 ? cvtpk(ge, ge) : 0u;
      LDS_DRAIN();
      FL[FL_H] = 1u;
    }
    STAMP(5);
    VM_CLEAR();
  } else if (wave == W_C) {
    const unsigned tag32 = __builtin_amdgcn_raw_buffer_load_b32(rws, 0, 0, AUX_SC1) + 1u, tag = tag32 & TAG_MASK;
    lds_wait<1>(FL + FL_T0 + 1, 1u);
    unsigned t0 = __builtin_amdgcn_readfirstlane(FL[FL_T0]);
    const unsigned town = t0;
    auto mean_offset = [&](int off) {
      const v2u tt = __builtin_amdgcn_raw_buffer_load_b64(rws, WS_TT + off + c * 256, 0, AUX_SC1);
      const bool ok = tt.y == tag32;
      const float dsum = row_sum(ok ? (float)(int)(tt.x - town) : 0.f), dcnt = row_sum(ok ? 1.f : 0.f);
      float dv = ok ? (float)(int)(tt.x - town) : -1000.f;
      dv = fminf(dv, dpp<XOR1>(dv));
      const float dm = fmaxf(dsum * __builtin_amdgcn_rcpf(fmaxf(dcnt, 1.f)), row_max(dv) - (float)CAP);
      return __builtin_amdgcn_readfirstlane((int)fminf(fmaxf(dm, -150.f), 150.f));
    };
    if (s == 0) {
      lds_wait<1>(FL + FL_TS + 1, 1u);
      if (lane < 8) __builtin_amdgcn_raw_buffer_store_b64(v2u{FL[FL_TS], tag32}, rws, WS_TT + 128 + lane * 8, h * 256, AUX_SC1);
    }
    pace<TS>(t0);
    t0 = town + mean_offset(128);
    *(volatile LAS v2u*)(FL + FL_T1) = v2u{t0, 2u};
    STAMP(1);
    lds_wait<4>(FL + FL_FW, 1u);
    const int hh = lane >> 5;
    v4f fw = *(LAS v4f*)(L + L_FW + hh * 4);
    int oaddr = WS_O + j * O_STRIDE + (hh * 128 + c * 8 + (g4 & 1) * 4) * 4;
    const v2u mlp = *(LAS v2u*)(L + L_G + (lane & 1) * 2);
    v4u mls = seal(v4f{asf(mlp.x), asf(mlp.y), asf(mlp.x), asf(mlp.y)}, tag);
    int maddr = lane < 4 ? WS_O + j * O_STRIDE + 1024 + lane * 16 : OOB;
    PIN(fw); PIN(oaddr); PIN(mls); PIN(maddr);
    pace<slot_time(44)>(t0);
    STAMP(2);
    v4f o;
#pragma nounroll
    for (int sp = 0; sp < SPIN_L; ++sp) {
      CBAR();
      v4u d[4];
#pragma unroll
      for (int w = 0; w < 4; ++w) d[w] = *(LAS v4u*)(L + L_ORED + w * 256 + lane * 4);
      o = __builtin_bit_cast(v4f, d[0]) * fw[0];
#pragma unroll
      for (int w = 1; w < 4; ++w) o += __builtin_bit_cast(v4f, d[w]) * fw[w];
      const float chk = (o[0] + o[1]) + (o[2] + o[3]);
      if (__all(chk == chk)) break;
      if (__all(full(d[0]) & full(d[1]) & full(d[2]) & full(d[3]))) break;
    }
    STAMP(3);
    st128(seal(o, tag), rws, oaddr, 0);
    st128(mls, rws, maddr, 0);
    STAMP(4);
    pace<TO>(now());
    FL[FL_OPUB] = 1u;
    VM_CLEAR();
  } else {
    const int aw = wave - W_A;
    *(LAS v4u*)(L + L_ORED + aw * 256 + lane * 4) = v4u{EMPTY, EMPTY, EMPTY, EMPTY};
    if (aw != 0) {
      __builtin_amdgcn_s_sleep(TOUCH);
      if (aw == 1) { touch2(vc, kvo, kvo); touch2(out, 0, 0); }
      else touch2(aw == 2 ? wo : w2, j * (aw == 2 ? 18432 : 20736), j * (aw == 2 ? 18432 : 20736) + 18400);
    }
    const bool reducer = aw == 1 && (j & 31) < 9;
    const int hb = c >> 3;
    lds_wait<4>(FL + FL_PROG + aw, 2u);
    lds_wait<1>(FL + FL_PROG + aw, 3u);
    STAMP(1);
    v4u kf[16];
#pragma unroll
    for (int t = 0; t < 4; ++t)
#pragma unroll
      for (int kk = 0; kk < 4; ++kk)
        kf[t * 4 + kk] = *(LAS v4u*)(L + L_K + (aw * 16 + t * 4 + (c & 3)) * 256 + (c >> 2) * 64 + (((kk * 4 + g4) ^ c) * 4));
#pragma unroll
    for (int i = 0; i < 16; ++i) PIN(kf[i]);
    lds_wait<0>(FL + FL_Q, 1u);
    STAMP(2);
    v4u qf[4];
#pragma unroll
    for (int kk = 0; kk < 4; ++kk) qf[kk] = *(LAS v4u*)(L + L_QS + hb * 64 + (kk * 4 + g4) * 4);
    SB();
    v4f sc[4];
#pragma unroll
    for (int t = 0; t < 4; ++t) sc[t] = v4f{0.f, 0.f, 0.f, 0.f};
#pragma unroll
    for (int t = 0; t < 4; ++t)
#pragma unroll
      for (int kk = 0; kk < 4; ++kk) sc[t] = mfma(kf[t * 4 + kk], qf[kk], sc[t]);
    float sv[4];
#pragma unroll
    for (int e = 0; e < 4; ++e) {
      const float lo = (c & 1) ? sc[1][e] : sc[0][e], hi = (c & 1) ? sc[3][e] : sc[2][e];
      sv[e] = rbf(rbf((c & 2) ? hi : lo) * 0.08838834764831845f);
    }
    float m = fmaxf(fmaxf(sv[0], sv[1]), fmaxf(sv[2], sv[3]));
    m = fmaxf(m, dpp<XOR1>(m)); m = fmaxf(m, dpp<XOR2>(m));
    m = xrow_max(m);
    float p[4];
    v4u pk;
#pragma unroll
    for (int e = 0; e < 4; ++e) { p[e] = __builtin_amdgcn_exp2f((sv[e] - m) * LOG2E); pk[e] = cvtpk(p[e], p[e]); }
    *(LAS v4u*)(L + L_P + aw * 128 + (hb * 4 + g4) * 16 + (c & 3) * 4) = pk;
    float l = (p[0] + p[1]) + (p[2] + p[3]);
    l += dpp<XOR1>(l); l += dpp<XOR2>(l);
    l = xrow_sum(l);
    if ((lane & 55) == 0) *(LAS v2u*)(L + L_ML + aw * 4 + hb * 2) = v2u{asu(m), asu(l)};
    LDS_DRAIN();
    FL[FL_P + aw] = 1u;
    STAMP(3);
    const unsigned tag32 = L[L_XR + 512] + 1u, tag = tag32 & TAG_MASK;
    if (aw == 0) {
      lds_wait4<0>(FL + FL_P, 1u);
      const int hh = (lane >> 2) & 1, w = lane & 3;
      const v2u ml = *(LAS v2u*)(L + L_ML + w * 4 + hh * 2);
      float M = asf(ml.x);
      M = fmaxf(M, dpp<XOR1>(M)); M = fmaxf(M, dpp<XOR2>(M));
      const float fw = __builtin_amdgcn_exp2f((asf(ml.x) - M) * LOG2E);
      float Ls = asf(ml.y) * fw;
      Ls += dpp<XOR1>(Ls); Ls += dpp<XOR2>(Ls);
      if (lane < 8) L[L_FW + lane] = asu(fw);
      if ((lane & 59) == 0) *(LAS v2u*)(L + L_G + hh * 2) = v2u{asu(M), asu(Ls)};
      LDS_DRAIN();
      FL[FL_FW] = 1u;
      STAMP(4);
    } else if (reducer) {
      const int red = (j >> 5) * 9 + (j & 31);
      const int ocol = red * 8 + (lane & 1) * 4 + g4;
      const unsigned xb = ((LAS unsigned short*)(L + L_XR))[ocol];
      const unsigned bb = ((LAS unsigned short*)(L + L_XR + 768))[ocol & 7];
      float base = asf(xb << 16) + asf(bb << 16);
      PIN(base);
      lds_wait<4>(FL + FL_OPUB, 1u);
      lds_wait<0>(FL + FL_FPUB, 5u);
      pace<TF>(now());
      STAMP(4);
      v4f sum;
      bool done = false;
#pragma nounroll
      for (int sp = 0; sp < SPIN_G; ++sp) {
        CBAR(); COUNT();
        if (!done) {
          v4u pv[8];
#pragma unroll
          for (int i = 0; i < 8; ++i) pv[i] = __builtin_amdgcn_raw_buffer_load_b128(rws, (lane >> 1) * 64 + (lane & 1) * 16 + (i & 1) * 2048, WS_F + (red >> 1) * F_STRIDE + (red & 1) * 32 + (i >> 1) * 4096, AUX_SC1);
          unsigned bad = 0;
          sum = unseal(pv[0], tag, bad);
#pragma unroll
          for (int i = 1; i < 8; ++i) sum += unseal(pv[i], tag, bad);
          done = (bad & TAG_MASK) == 0u;
        }
        if (__all(done)) break;
      }
      STAMP(5);
      auto s02 = __builtin_amdgcn_permlane32_swap(asu(sum[0]), asu(sum[2]), false, false);
      auto s13 = __builtin_amdgcn_permlane32_swap(asu(sum[1]), asu(sum[3]), false, false);
      const float u0 = asf(s02[0]) + asf(s02[1]), u1 = asf(s13[0]) + asf(s13[1]);
      auto s01 = __builtin_amdgcn_permlane16_swap(asu(u0), asu(u1), false, false);
      float u = asf(s01[0]) + asf(s01[1]);
      u += dpp<ROR8>(u); u += dpp<ROR4>(u); u += dpp<XOR2>(u);
      u += base;
      if ((lane & 14) == 0) __builtin_amdgcn_raw_buffer_store_b16((unsigned short)cvtpk(u, u), RS(out, D * 2), ocol * 2, 0, 0);
      if (j == 0 && lane == 0) __builtin_amdgcn_raw_buffer_store_b32(tag32, rws, 0, 0, AUX_SC1);
      STAMP(6);
    }
    VM_CLEAR();
  }

}

extern "C" __device__ __attribute__((used, noinline)) void fused_decode_end() {}

extern "C" void kernel_launch(void* const* d_in, const int* in_sizes, int n_in, void* d_out, int out_size, void* d_ws, size_t ws_size,
                              hipStream_t stream) {
  fused_decode<<<256, NWAVE * 64, 0, stream>>>(d_in[3], d_in[1], d_in[6], d_in[2], d_in[4], d_in[8], d_ws, d_in[0], d_in[7], d_in[9], d_out);
}
```

```cpp
#include <hip/hip_runtime.h>
#include <stdint.h>
#include <type_traits>

typedef unsigned v4u __attribute__((ext_vector_type(4)));
typedef unsigned v2u __attribute__((ext_vector_type(2)));
typedef float v4f __attribute__((ext_vector_type(4)));
typedef float v2f __attribute__((ext_vector_type(2)));
typedef short v4s __attribute__((ext_vector_type(4)));
typedef __bf16 bf16x8 __attribute__((ext_vector_type(8)));
typedef __bf16 bf16x2 __attribute__((ext_vector_type(2)));
#define LAS __attribute__((address_space(3)))

constexpr int D = 576, SK = 16384, WIN = 4096, F = 4608;
constexpr int KV_BYTES = 16 * SK * 128 * 2;
constexpr int WQ_BYTES = D * 4096 * 2, W1_BYTES = D * F * 2;
constexpr int WS_TT = 4096  , WS_Q = 8192, WS_H = WS_Q + 256 * 256, WS_O = WS_H + 72 * 5 * 256, O_STRIDE = 1152,
              WS_F = WS_O + 256 * O_STRIDE, F_STRIDE = 256 * 64 + 256, WS_END = WS_F + 36 * F_STRIDE;
constexpr unsigned EMPTY = 0xFFFFFFFFu;
constexpr int OOB = 0x7FFFFFFF;
#define WINDOW 11
#define PACE10 148
#define PACE0 80
#define BLIND 3
#define AHEAD 6
#define LAG 3
#define LAG1 4
#define LAG2 7
#define LAG3 5
#define TQ (slot_time(27))
#define TH (slot_time(31))
#define TM (slot_time(48))
#define EARLY AHEAD
#define DRIFT0 (SMP + 19)
#define TS (slot_time(SMP + 13))
#define SYNC 1
#define OATOM 0
#define TOUCH 60
#define EXTRA 0
#define STAG 0
#define PV0 42
#define TG 15
#define F4S 43
#define F4D 1
#define FCOST 92
#define SMP 12
#define RTT0 55
#define HAT 47
#define WE 11
#define WEN 28
#define NQ 27
#define NH 33
#define TA (slot_time(9))
#define GAP 200
#define KVORD 1
#define CAP 35
#define OG 49
#define SETTLE 60
#define TO 35
#define TF 25
constexpr int AUX_NT = 2, AUX_SC1 = 16, W = WINDOW;
#define ABL 0
constexpr int SPIN_G = ABL ? 1 : 20000, SPIN_L = 400000;
constexpr float LOG2E = 1.4426950408889634f;

constexpr int L_WQ = 0  , L_K = 5120  , L_W1 = L_K + 16384  , L_FLAG = L_W1 + 6144  ;
constexpr int FL_Q = 0, FL_H = 1, FL_PROG = 4  , FL_P = 8  , FL_KDONE = 13, FL_FW = 14, FL_G = 15, FL_VDONE = 16, FL_OPUB = 17,
              FL_FPUB = 18, FL_T0 = 20  , FL_T1 = 22  , FL_TS = 24  , FL_SLOT = 26;
constexpr int L_XR = L_FLAG + 32  , L_Y = L_XR + 2 * 1024  , L_QS = L_Y + 2 * 512  , L_HS = L_QS + 128  ,
              L_ML = L_HS + 32  , L_FW = L_ML + 16  , L_G = L_FW + 16  , L_OS = L_G + 16  , L_ORED = L_OS + 16  ,
              L_P = L_ORED + 1280  , L_END = L_P + 512;
constexpr int YZ = 192;

#define RS(p, n) __builtin_amdgcn_make_buffer_rsrc((void*)(p), 0, (int)(n), 0x00020000)
#define SB() __builtin_amdgcn_sched_barrier(0)
#define CBAR() asm volatile("" ::: "memory")
#define LDS_DRAIN() asm volatile("s_waitcnt lgkmcnt(0)" ::: "memory")
#define VM_DRAIN() asm volatile("s_waitcnt vmcnt(0)" ::: "memory")
#define VM_CLEAR() __builtin_amdgcn_s_waitcnt(0x0F70)
#define PIN(x) asm volatile("" : "+v"(x))

#define COUNT() do {} while (0)
#define STAMP(i) do { if ((i) == 7 && t_ev == 0) t_ev = now(); } while (0)

__device__ __forceinline__ float asf(unsigned u) { return __builtin_bit_cast(float, u); }
__device__ __forceinline__ unsigned asu(float f) { return __builtin_bit_cast(unsigned, f); }
__device__ __forceinline__ unsigned cvtpk(float lo, float hi) {
  return __builtin_bit_cast(unsigned, __builtin_convertvector(v2f{lo, hi}, bf16x2));
}
__device__ __forceinline__ v2f unpk(unsigned p) { return v2f{asf(p << 16), asf(p & 0xFFFF0000u)}; }
__device__ __forceinline__ float rbf(float f) { return asf(cvtpk(0.f, f)); }
__device__ __forceinline__ float vmax(float a, float b) { float r; asm("v_max_f32 %0, %1, %2" : "=v"(r) : "v"(a), "v"(b)); return r; }
__device__ __forceinline__ float vmax3(float a, float b, float c) { float r; asm("v_max3_f32 %0, %1, %2, %3" : "=v"(r) : "v"(a), "v"(b), "v"(c)); return r; }
template <int CTRL> __device__ __forceinline__ float dpp(float x) {
  return asf(__builtin_amdgcn_mov_dpp(asu(x), CTRL, 0xf, 0xf, true));
}
constexpr int XOR1 = 0xB1, XOR2 = 0x4E, XOR7 = 0x141, ROR8 = 0x128, ROR4 = 0x124;
__device__ __forceinline__ float xrow_sum(float x) {
  auto s = __builtin_amdgcn_permlane16_swap(asu(x), asu(x), false, false);
  x = asf(s[0]) + asf(s[1]);
  auto t = __builtin_amdgcn_permlane32_swap(asu(x), asu(x), false, false);
  return asf(t[0]) + asf(t[1]);
}
__device__ __forceinline__ float xrow_max(float x) {
  auto s = __builtin_amdgcn_permlane16_swap(asu(x), asu(x), false, false);
  x = fmaxf(asf(s[0]), asf(s[1]));
  auto t = __builtin_amdgcn_permlane32_swap(asu(x), asu(x), false, false);
  return fmaxf(asf(t[0]), asf(t[1]));
}
__device__ __forceinline__ float row_sum(float x) {
  x += dpp<XOR1>(x); x += dpp<XOR2>(x); x += dpp<XOR7>(x); x += dpp<ROR8>(x);
  return x;
}
__device__ __forceinline__ float row_max(float x) {
  asm("s_nop 1\n\tv_max_f32_dpp %0, %0, %0 quad_perm:[1,0,3,2] row_mask:0xf bank_mask:0xf\n\ts_nop 1\n\t"
      "v_max_f32_dpp %0, %0, %0 quad_perm:[2,3,0,1] row_mask:0xf bank_mask:0xf\n\ts_nop 1\n\t"
      "v_max_f32_dpp %0, %0, %0 row_half_mirror row_mask:0xf bank_mask:0xf\n\ts_nop 1\n\t"
      "v_max_f32_dpp %0, %0, %0 row_ror:8 row_mask:0xf bank_mask:0xf" : "+v"(x));
  return x;
}
__device__ __forceinline__ v4f mfma(v4u a, v4u b, v4f c) {
  return __builtin_amdgcn_mfma_f32_16x16x32_bf16(__builtin_bit_cast(bf16x8, a), __builtin_bit_cast(bf16x8, b), c, 0, 0, 0);
}
__device__ __forceinline__ v4u box(v4f v) {
  v4u r;
#pragma unroll
  for (int i = 0; i < 4; ++i) r[i] = min(asu(v[i]), 0xFFFFFFFEu);
  return r;
}
constexpr unsigned TAG_MASK = 0x3FFu;
__device__ __forceinline__ v4u seal(v4f v, unsigned tag) {
  v4u r;
#pragma unroll
  for (int i = 0; i < 4; ++i) r[i] = (asu(v[i]) & ~TAG_MASK) | tag;
  return r;
}
__device__ __forceinline__ v4f unseal(v4u w, unsigned tag, unsigned& bad) {
  const v4u x = w ^ tag;
  bad |= (x.x | x.y) | (x.z | x.w);
  return __builtin_bit_cast(v4f, x);
}
__device__ __forceinline__ bool full(v4u v) { return max(max(v.x, v.y), max(v.z, v.w)) != EMPTY; }
template <int NAP = 2> __device__ __forceinline__ void lds_wait(volatile LAS unsigned* f, unsigned need) {
#pragma nounroll
  for (int sp = 0; sp < SPIN_L; ++sp) {
    if ((unsigned)__builtin_amdgcn_readfirstlane(*f) >= need) break;
    if (NAP) __builtin_amdgcn_s_sleep(NAP);
  }
  CBAR();
}
template <int NAP> __device__ __forceinline__ void lds_wait4(volatile LAS unsigned* f, unsigned need) {
#pragma nounroll
  for (int sp = 0; sp < SPIN_L; ++sp) {
    const v4u v = *(volatile LAS v4u*)f;
    if ((unsigned)__builtin_amdgcn_readfirstlane(min(min(v.x, v.y), min(v.z, v.w))) >= need) break;
    if (NAP) __builtin_amdgcn_s_sleep(NAP);
  }
  CBAR();
}
__device__ __forceinline__ void lds_inc(LAS unsigned* f, int lane) {
  if (lane == 0) __hip_atomic_fetch_add(f, 1u, __ATOMIC_RELAXED, __HIP_MEMORY_SCOPE_WORKGROUP);
}
typedef __attribute__((address_space(1))) unsigned long long gu64;
__device__ __forceinline__ v2u peek(void* ws, int off) {
  unsigned long long zero = 0ull;
  asm volatile("" : "+v"(zero));
  const unsigned long long r = __hip_atomic_fetch_or((gu64*)((char*)ws + off), zero, __ATOMIC_RELAXED, __HIP_MEMORY_SCOPE_AGENT);
  return v2u{(unsigned)r, (unsigned)(r >> 32)};
}
__device__ __forceinline__ void touch_and_barrier(const void* p, unsigned o0, unsigned o1, unsigned o2, const void* q, unsigned o3) {
  unsigned d0, d1, d2, d3;
  asm volatile("s_load_dword %0, %4, %5\n\ts_load_dword %1, %4, %6\n\ts_load_dword %2, %4, %7\n\ts_load_dword %3, %8, %9\n\ts_barrier\n\ts_waitcnt lgkmcnt(0)"
               : "=&s"(d0), "=&s"(d1), "=&s"(d2), "=&s"(d3) : "s"(p), "s"(o0), "s"(o1), "s"(o2), "s"(q), "s"(o3) : "memory");
}
__device__ __forceinline__ void touch2(const void* p, unsigned o0, unsigned o1) {
  unsigned d0, d1;
  asm volatile("s_load_dword %0, %2, %3\n\ts_load_dword %1, %2, %4\n\ts_waitcnt lgkmcnt(0)" : "=&s"(d0), "=&s"(d1) : "s"(p), "s"(o0), "s"(o1) : "memory");
}
__device__ __forceinline__ unsigned now() {
  unsigned long long t;
  asm volatile("s_memrealtime %0\n\ts_waitcnt lgkmcnt(0)" : "=s"(t)::"memory");
  return (unsigned)t;
}
template <int T> __device__ __forceinline__ void pace(unsigned t0) {
  asm volatile("1:\n\ts_memrealtime s[90:91]\n\ts_waitcnt lgkmcnt(0)\n\ts_sub_u32 s90, s90, %0\n\ts_cmpk_lt_u32 s90, %1\n\ts_cbranch_scc1 1b"
               ::"s"(__builtin_amdgcn_readfirstlane(t0)), "i"(T) : "memory", "scc", "s90", "s91");
}
constexpr int slot_cost(int n) { return n == 10 ? 30 : n == 42 ? GAP : (n >= 43 ? (n == 47 ? 60 : FCOST) : 100); }
constexpr int slot_time(int n) {
  if (n < AHEAD) return (n - BLIND + 1) * 15;
  int c = 0;
  for (int i = AHEAD; i < n; ++i) c += slot_cost(i);
  return PACE0 + c * PACE10 / 1000;
}
constexpr int pv_done(int n) { return n < PV0 ? 0 : ((n - PV0) * 5 + 2 < n - 26 - LAG ? (n - PV0) * 5 + 2 : n - 26 - LAG); }
template <int N> __device__ __forceinline__ void vmwait() { asm volatile("s_waitcnt vmcnt(%0)" ::"i"(N) : "memory"); }
__device__ __forceinline__ void st128(v4u d, __amdgpu_buffer_rsrc_t r, int voff, int soff) {
  __builtin_amdgcn_raw_buffer_store_b128(d, r, voff, soff, AUX_SC1);
  asm volatile("s_nop 1" ::"v"(d) : "memory");
}
__device__ __forceinline__ void dma(__amdgpu_buffer_rsrc_t r, int voff, int soff, int ldsaddr) {
  asm volatile("s_mov_b32 m0, %0\n\ts_nop 0\n\tbuffer_load_dwordx4 %1, %2, %3 offen nt lds" ::"s"(ldsaddr), "v"(voff), "s"(r), "s"(soff) : "memory");
}
__device__ __forceinline__ void dma_dev(__amdgpu_buffer_rsrc_t r, int voff, int ldsaddr) {
  asm volatile("s_mov_b32 m0, %0\n\ts_nop 0\n\tbuffer_load_dwordx4 %1, %2, 0 offen sc1 lds" ::"s"(ldsaddr), "v"(voff), "s"(r) : "memory");
}
__device__ __forceinline__ v4u keep_mask(int m) {
  v4u k;
#pragma unroll
  for (int v = 0; v < 4; ++v) k[v] = ((m >> 1) == v) ? ((m & 1) ? 0xFFFF0000u : 0x0000FFFFu) : 0u;
  return k;
}
template <int I, int N, class Fn> __device__ __forceinline__ void static_for(Fn&& fn) {
  if constexpr (I < N) { fn(std::integral_constant<int, I>{}); static_for<I + 1, N>(fn); }
}
__device__ __forceinline__ void layernorm(unsigned (&p)[5], int lane) {
  const float c0 = asf(__builtin_amdgcn_readfirstlane(p[0]) << 16);
  v2f v[5], d[5];
#pragma unroll
  for (int i = 0; i < 5; ++i) { v[i] = unpk(p[i]); d[i] = v[i] - c0; }
  if (lane >= 32) d[4] = v2f{0.f, 0.f};
  const v2f s1v = (d[0] + d[1]) + (d[2] + d[3]) + d[4];
  v2f s2v = d[0] * d[0];
#pragma unroll
  for (int i = 1; i < 5; ++i) s2v = __builtin_elementwise_fma(d[i], d[i], s2v);
  const float s1 = xrow_sum(row_sum(s1v.x + s1v.y)) * (1.0f / D), s2 = xrow_sum(row_sum(s2v.x + s2v.y)) * (1.0f / D);
  const float var = fmaxf(fmaf(-s1, s1, s2), 0.f);
  const float mb = rbf(c0 + s1), sd = rbf(__builtin_amdgcn_sqrtf(rbf(rbf(var) + 1e-5f)));
  const float inv = __builtin_amdgcn_rcpf(sd);
#pragma unroll
  for (int i = 0; i < 5; ++i) {
    const v2f t = v[i] - mb;
    const v2f u = v2f{rbf(t.x), rbf(t.y)} * inv;
    p[i] = cvtpk(u.x, u.y);
  }
}

constexpr int W_F4 = 4, W_C = 5, W_P = 6, W_A = 8, NWAVE = 12;

extern "C" __global__ void __launch_bounds__(NWAVE * 64, 3)
fused_decode(const void* __restrict__ wq, const void* __restrict__ kc, const void* __restrict__ w1, const void* __restrict__ vc,
             const void* __restrict__ wo, const void* __restrict__ w2, void* __restrict__ ws, const void* __restrict__ x,
             const void* __restrict__ b1, const void* __restrict__ b2, void* __restrict__ out) {
  __shared__ __attribute__((aligned(16))) unsigned smem[L_END];
  LAS unsigned* L = (LAS unsigned*)smem;
  volatile LAS unsigned* FL = (volatile LAS unsigned*)smem + L_FLAG;
  const int tid = threadIdx.x, lane = tid & 63, wave = __builtin_amdgcn_readfirstlane(tid >> 6);
  const int b = blockIdx.x, j = (b & 7) * 32 + (b >> 3), h = j >> 4, s = j & 15;
  const int r8 = lane >> 3, c8 = lane & 7, g4 = lane >> 4, c = lane & 15;
  const int lcA = (j * 9) >> 5, rowA = 18 * ((j * 9) & 31);
  const int lds0 = (int)(unsigned)(uintptr_t)L;
  unsigned t_ev = 0;
  if (!(ABL & 64) && (wave >> 1) == (W_P >> 1)) {
    const int dst = lds0 + (L_XR + (wave & 1) * 1024) * 4;
    dma_dev(RS(x, D * 2), lane * 16, dst);
    dma_dev(RS(x, D * 2), lane * 16 + 1024, dst + 1024);
    dma_dev(RS(ws, 16), lane * 16, dst + 2048);
    const int red8 = ((j >> 5) * 9 + min(j & 31, 8)) * 16;
    dma_dev(RS((wave & 1) ? (const char*)b1 : (const char*)b2, (wave & 1) ? F * 2 : D * 2), lane < 4 ? lane * 16 + ((wave & 1) ? (j * 36) & ~15 : red8) : OOB, dst + 3072);
  }
  if (!(ABL & 128) && wave == W_C) {
    unsigned long long pc;
    unsigned pcoff, len;
    asm volatile("s_getpc_b64 %0\n.Lpc%=:\n\ts_mov_b32 %1, .Lpc%=-fused_decode\n\ts_mov_b32 %2, fused_decode_end-fused_decode"
                 : "=s"(pc), "=s"(pcoff), "=s"(len));
    dma_dev(RS(pc - pcoff, len), lane < 40 ? lane * 16 + (b >> 3) * 640 : OOB, lds0 + (L_ORED + 1024) * 4);
  }
  const unsigned kvo = (h * SK + (SK - WIN) + s * 16) * 256;
  if (wave == W_A) {
    const unsigned c1 = lcA * 128;
    touch_and_barrier(w1, rowA * 9216 + c1, min(rowA + 161, D - 1) * 9216 + c1, c1, kc, kvo);
  } else if (wave >= 4) {
    if (wave == W_F4 && lane < 32) L[L_FLAG + lane] = 0u;
    LDS_DRAIN();
    __builtin_amdgcn_s_barrier();
    CBAR();
  }
  const auto rws = RS(ws, WS_END);
  const auto rwq = RS(ws, (ABL & 4) ? 0 : WS_END), rwh = RS(ws, (ABL & 8) ? 0 : WS_END), rwm = RS(ws, (ABL & 16) ? 0 : WS_END), rwp = RS(ws, (ABL & 32) ? 0 : WS_END);

  if (__builtin_expect(wave <= W_F4, 1)) {
    v4u wf[9], ha[5], mskf, mw, ow;
    int vo, v2, faddr, oga, mga;
    unsigned tag;
    auto prep_addr = [&]() {
      const bool live = wave < 4 || c < 8;
      vo = live ? g4 * 4608 + wave * 256 + c * 16 : OOB - 4096;
      v2 = live ? g4 * 1152 + wave * 256 + c * 16 : OOB - 5 * 4608;
      faddr = lane < 32 && live ? WS_F + (wave * 8 + (c >> 1)) * F_STRIDE + j * 64 + (c & 1) * 32 + g4 * 16 : OOB;
      oga = WS_O + (h * 16 + c) * O_STRIDE + s * 64 + g4 * 16;
      mga = WS_O + 1024 + (h * 16 + c) * O_STRIDE + (s >> 3) * 16;
      mskf = keep_mask(c);
      PIN(mga); PIN(vo); PIN(v2); PIN(faddr); PIN(oga); PIN(mskf);
    };
    auto prep_h = [&]() {
      lds_wait<0>(FL + FL_H, 1u);
      unsigned hv[5];
      const unsigned ep = L[L_XR + 512];
#pragma unroll
      for (int rg = 0; rg < 5; ++rg) hv[rg] = L[L_HS + rg * 4 + g4];
      SB();
      tag = (ep + 1u) & TAG_MASK;
#pragma unroll
      for (int rg = 0; rg < 5; ++rg) ha[rg] = mskf & hv[rg];
      asm volatile("" : "+v"(tag), "+v"(ha[0]), "+v"(ha[1]), "+v"(ha[2]), "+v"(ha[3]), "+v"(ha[4]));
    };
    if (__builtin_expect(wave < 4, 1)) {
      const auto rq = RS(wq, WQ_BYTES), rk = RS(kc, KV_BYTES), rv = RS(vc, KV_BYTES), r1 = RS(w1, W1_BYTES);
      const int qs = (s >> 2) * 144 * 8192 + h * 512 + (s & 3) * 128;
      const int kvb = (h * SK + (SK - WIN) + s * 16) * 256 + wave * 1024, rot = h;
      const int cs = (c8 ^ (r8 & 6)) * 16;
      __amdgpu_buffer_rsrc_t ro, r2;
      v4u vv[16], pk[4], mskv, alast;
      v4f acc = {0.f, 0.f, 0.f, 0.f};
      unsigned t0;
      int drift = 0;
      static_for<0, 52>([&](auto nc) {
        constexpr int n = nc;
        if (n == 1) t0 = now();
        if (n == BLIND) __builtin_amdgcn_s_barrier();
        if (n == AHEAD) {
          vmwait<AHEAD - 1>();
          const unsigned t1 = now() - slot_time(n);
          if ((int)(t1 - t0) > 0) t0 = t1 + min((int)(t1 - t0), EXTRA);
          if (wave == 0) *(volatile LAS v2u*)(FL + FL_T0) = v2u{t0, 1u};
        }
        if (n < WEN) { if (n >= WE) vmwait<WE - 1>(); } else vmwait<W - 1>();
        if constexpr (n >= 43) pace<slot_time(n)>(t0 + drift);
        else if constexpr (n >= DRIFT0) pace<slot_time(n)>(t0 + ((drift * ((n - DRIFT0 + 1) * 256 / (43 - DRIFT0))) >> 8));
        else if constexpr (n >= BLIND) pace<slot_time(n)>(t0);
        if constexpr (n < 5) {
          dma(rq, min((n * 4 + wave) * 8 + r8, 143) * 8192 + cs, qs, lds0 + L_WQ * 4 + (n * 4 + wave) * 1024);
        } else if constexpr (n < 11) {
          constexpr int i = n - 5;
          const unsigned v = (unsigned)((rowA + min((i * 4 + wave) * 8 + r8, 161)) * 9216 + cs);
          dma(r1, (int)min(v, v - (unsigned)(W1_BYTES - 128)), lcA * 128, lds0 + L_W1 * 4 + (i * 4 + wave) * 1024);
        } else if constexpr (n < 27) {
          constexpr int i = n - 11;
          dma(rk, g4 * 256 + ((c ^ (g4 * 4 + (i & 3))) * 16), kvb + (((KVORD ? (i & 3) * 4 + (i >> 2) : i) + rot) & 15) * 65536, lds0 + L_K * 4 + (wave * 16 + i) * 1024);
        } else if constexpr (n < 43) {
          constexpr int i = n - 27;
          vv[i] = __builtin_amdgcn_raw_buffer_load_b128(rv, lane * 16, kvb + (((KVORD ? (i & 3) * 4 + (i >> 2) : i) + rot) & 15) * 65536, AUX_NT);
        } else if constexpr (n < 48) {
          wf[n - 43] = __builtin_amdgcn_raw_buffer_load_b128(r2, v2 + (n - 43) * 4608, 0, AUX_NT);
        } else {
          wf[n - 43] = __builtin_amdgcn_raw_buffer_load_b128(ro, vo, (n - 48) * 1152, AUX_NT);
        }
        SB();
        if (n == AHEAD) STAMP(7);
        if (n == 4 + LAG1) { vmwait<LAG1>(); FL[FL_PROG + wave] = 1u; STAMP(1); }
        if (n == 10 + LAG2) { vmwait<LAG2>(); FL[FL_PROG + wave] = 2u; }
        if (n == 26 + LAG3) { vmwait<LAG3>(); FL[FL_PROG + wave] = 3u; STAMP(2); }
        if (n == SMP + 3 && wave == 0) {
          if (s == 0) {
            vmwait<3>();
            const unsigned t1 = now() - (slot_time(SMP) + RTT0);
            *(volatile LAS v2u*)(FL + FL_TS) = v2u{(int)(t1 - t0) > 0 ? t1 : t0, 1u};
          }
        }
        if (n == 21) prep_addr();
        if (n == 22) {
          mskv = keep_mask(c8);
          PIN(mskv);
          ro = RS((const char*)wo + j * 18432, 18432);
          r2 = RS((const char*)w2 + j * 20736, 20736);
          asm volatile("" : "+s"(ro), "+s"(r2));
        }
        if ((n == NQ || n == NH) && wave == 0) FL[FL_SLOT] = n;
        if (n == DRIFT0 - 1 || n == 36) {
          const v2u t1 = *(volatile LAS v2u*)(FL + FL_T1);
          drift = __builtin_amdgcn_readfirstlane(t1.y) ? (int)(__builtin_amdgcn_readfirstlane(t1.x) - t0) : 0;
        }
        if (n == PV0) {
          lds_wait<0>(FL + FL_P + wave, 1u);
          STAMP(3);
#pragma unroll
          for (int t = 0; t < 4; ++t) pk[t] = *(LAS v4u*)(L + L_P + wave * 128 + ((c >> 3) * 4 + g4) * 16 + t * 4);
          alast = mskv & pk[3][3];
          PIN(alast);
        }
        if constexpr (n >= PV0 && n <= 42 + LAG) {
          constexpr int done = pv_done(n - 1), ready = pv_done(n);
#pragma unroll
          for (int i = done; i < ready; ++i) acc = mfma(i == 15 ? alast : mskv & pk[i >> 2][i & 3], vv[i], acc);
          if (n == 42 + LAG) {
            *(LAS v4u*)(L + L_ORED + wave * 256 + lane * 4) = box(acc);
            STAMP(4);
          }
        }
        if (n == HAT) prep_h();
        if (n == OG) {
          lds_wait<0>(FL + FL_OPUB, 1u);
          STAMP(5);
          mw = __builtin_amdgcn_raw_buffer_load_b128(rws, mga, 0, AUX_SC1);
          ow = __builtin_amdgcn_raw_buffer_load_b128(rws, oga, 0, AUX_SC1);
        }
        SB();
      });
    } else {
      prep_addr();
      const auto ro = RS((const char*)wo + j * 18432, 18432), r2 = RS((const char*)w2 + j * 20736, 20736);
      lds_wait<4>(FL + FL_T1 + 1, 2u);
      const unsigned t0 = __builtin_amdgcn_readfirstlane(FL[FL_T1]);
      static_for<0, 9>([&](auto nc) {
        constexpr int n = nc;
        pace<slot_time(F4S + n * F4D)>(t0);
        if constexpr (n < 5) asm volatile("buffer_load_dwordx4 %0, %1, %2, 0 offen nt" : "=v"(wf[n]) : "v"(v2 + n * 4608), "s"(r2) : "memory");
        else asm volatile("buffer_load_dwordx4 %0, %1, %2, 0 offen offset:%3 nt" : "=v"(wf[n]) : "v"(vo), "s"(ro), "i"((n - 5) * 1152) : "memory");
        SB();
        if (n + 43 == HAT) prep_h();
        if (n + 43 == OG) {
          lds_wait<0>(FL + FL_OPUB, 1u);
          STAMP(5);
          asm volatile("buffer_load_dwordx4 %0, %2, %4, 0 offen sc1\n\tbuffer_load_dwordx4 %1, %3, %4, 0 offen sc1"
                       : "=&v"(mw), "=&v"(ow) : "v"(mga), "v"(oga), "s"(rws) : "memory");
        }
        SB();
      });
    }
    v4f a2 = {0.f, 0.f, 0.f, 0.f};
#pragma unroll
    for (int rg = 0; rg < 5; ++rg) a2 = mfma(ha[rg], wf[rg], a2);
    PIN(a2);
    SB();
    unsigned bad = 0;
    v4f ml = unseal(mw, tag, bad), u = unseal(ow, tag, bad);
    COUNT();
    if (__builtin_expect(!__all((bad & TAG_MASK) == 0u), 0)) {
#pragma nounroll
      for (int sp = 0; sp < SPIN_G; ++sp) {
        COUNT();
        CBAR();
        bad = 0;
        ml = unseal(__builtin_amdgcn_raw_buffer_load_b128(rws, mga, 0, AUX_SC1), tag, bad);
        u = unseal(__builtin_amdgcn_raw_buffer_load_b128(rws, oga, 0, AUX_SC1), tag, bad);
        if (__all((bad & TAG_MASK) == 0u)) break;
      }
    }
    STAMP(6);
    const float gw = __builtin_amdgcn_exp2f((ml[0] - row_max(ml[0])) * LOG2E);
    float r[5] = {u[0] * gw, u[1] * gw, u[2] * gw, u[3] * gw, ml[1] * gw};
#pragma unroll
    for (int i = 4; i >= 0; --i) r[i] += dpp<XOR1>(r[i]);
#pragma unroll
    for (int i = 4; i >= 0; --i) r[i] += dpp<XOR2>(r[i]);
#pragma unroll
    for (int i = 4; i >= 0; --i) r[i] += dpp<XOR7>(r[i]);
#pragma unroll
    for (int i = 4; i >= 0; --i) r[i] += dpp<ROR8>(r[i]);
    const float inv = __builtin_amdgcn_rcpf(r[4]);
    v4u oa[4];
#pragma unroll
    for (int rg = 0; rg < 4; ++rg) oa[rg] = mskf & cvtpk(r[rg] * inv, r[rg] * inv);
    SB();
#pragma unroll
    for (int rg = 0; rg < 4; ++rg) a2 = mfma(oa[rg], wf[5 + rg], a2);
    st128(seal(a2, tag), rws, faddr, 0);
    lds_inc(L + L_FLAG + FL_FPUB, lane);
    if (wave != 3) STAMP(7);
  } else if ((wave >> 1) == (W_P >> 1)) {
    const int pw = wave - W_P;
    LAS unsigned* Y = L + L_Y + pw * 512;
    *(LAS v4u*)(Y + lane * 8) = v4u{0u, 0u, 0u, 0u};
    *(LAS v4u*)(Y + lane * 8 + 4) = v4u{0u, 0u, 0u, 0u};
    const int nA = min(162, D - rowA), sh = pw == 0 ? 0 : (rowA & 2);
    const int ya = YZ + sh + (pw == 0 ? (s >> 2) * 144 : rowA), yb = pw == 0 ? ya : (nA == 162 ? 0 : YZ + sh - nA);
    const LAS char* ap = (const LAS char*)Y + ((c < 8 ? ya : yb) + g4 * 4) * 2;
    const LAS char* bp = (const LAS char*)(L + (pw == 0 ? L_WQ : L_W1)) + (g4 * 4 + (c >> 2)) * 128 + (c & 3) * 8;
    const int key = (g4 & 1) * 2 + (c >> 3);
    const int gA = pw == 0 ? WS_Q + j * 256 : WS_H + (lcA * 5 + j - (32 * lcA) / 9) * 256;
    int gout = g4 == 0 ? gA + c * 16 : (g4 == 2 && pw == 1 && nA < 162 ? WS_H + (lcA + 1) * 5 * 256 + c * 16 : OOB);
    PIN(gout);
    VM_DRAIN();
    STAMP(1);
    unsigned xp[5];
#pragma unroll
    for (int i = 0; i < 5; ++i) xp[i] = L[L_XR + pw * 1024 + i * 64 + lane];
    const unsigned tag = (L[L_XR + pw * 1024 + 512] + 1u) & TAG_MASK;
#pragma nounroll
    for (int rep = 0; rep <= pw; ++rep) layernorm(xp, lane);
#pragma unroll
    for (int i = 0; i < 5; ++i)
      if (i < 4 || lane < 32) Y[(YZ + sh) / 2 + i * 64 + lane] = xp[i];
    v4u af[6];
#pragma unroll
    for (int ks = 0; ks < 6; ++ks) {
      const v2u lo = *(const LAS v2u*)(ap + ks * 64), hi = *(const LAS v2u*)(ap + ks * 64 + 32);
      af[ks] = v4u{lo.x, lo.y, hi.x, hi.y};
    }
    if (pw == 0) { af[4].z = 0u; af[4].w = 0u; }
    af[5] = v4u{g4 == 0 ? af[5].x : 0u, 0u, 0u, 0u};
#pragma unroll
    for (int ks = 0; ks < 6; ++ks) PIN(af[ks]);
    STAMP(2);
    lds_wait4<1>(FL + FL_PROG, pw + 1u);
    v4f acc[4];
    {
      v4u bf[5][4];
#pragma unroll
      for (int ks = 0; ks < 5; ++ks)
#pragma unroll
        for (int cg = 0; cg < 4; ++cg) {
          const LAS char* q = bp + ((cg ^ key) * 32) + ks * 4096;
          const v2u lo = __builtin_bit_cast(v2u, __builtin_amdgcn_ds_read_tr16_b64_v4i16((LAS v4s*)q));
          const v2u hi = __builtin_bit_cast(v2u, __builtin_amdgcn_ds_read_tr16_b64_v4i16((LAS v4s*)(q + 2048)));
          bf[ks][cg] = v4u{lo.x, lo.y, hi.x, hi.y};
        }
      SB();
#pragma unroll
      for (int cg = 0; cg < 4; ++cg) acc[cg] = v4f{0.f, 0.f, 0.f, 0.f};
#pragma unroll
      for (int ks = 0; ks < 5; ++ks)
#pragma unroll
        for (int cg = 0; cg < 4; ++cg) acc[cg] = mfma(af[ks], bf[ks][cg], acc[cg]);
    }
    if (pw == 1) {
#pragma unroll
      for (int cg = 0; cg < 4; ++cg) {
        const v2u lo = __builtin_bit_cast(v2u, __builtin_amdgcn_ds_read_tr16_b64_v4i16((LAS v4s*)(bp + ((cg ^ key) * 32) + 5 * 4096)));
        acc[cg] = mfma(af[5], v4u{lo.x, lo.y, 0u, 0u}, acc[cg]);
      }
    }
    for (int i = 0; i < STAG * s; ++i) __builtin_amdgcn_s_sleep(1);
    st128(seal(v4f{acc[0][0], acc[1][0], acc[2][0], acc[3][0]}, tag), rwp, gout, 0);
    STAMP(3);
    const unsigned tpub = now();
    if (pw == 0) {
      pace<SETTLE>(tpub);
      lds_wait<2>(FL + FL_SLOT, NQ);
      v4f q;
#pragma nounroll
      for (int sp = 0; sp < SPIN_G; ++sp) {
        CBAR(); COUNT();
        v4u g[4];
#pragma unroll
        for (int i = 0; i < 4; ++i) g[i] = __builtin_amdgcn_raw_buffer_load_b128(rwq, lane * 16 + i * 1024, WS_Q + h * 4096, AUX_SC1);
        unsigned bad = 0;
        q = (unseal(g[0], tag, bad) + unseal(g[1], tag, bad)) + (unseal(g[2], tag, bad) + unseal(g[3], tag, bad));
        if (__all((bad & TAG_MASK) == 0u)) break;
      }
      STAMP(4);
      LAS unsigned short* qd = (LAS unsigned short*)(L + L_QS) + g4 * 64 + c;
      const unsigned q01 = cvtpk(q[0], q[1]), q23 = cvtpk(q[2], q[3]);
      qd[0] = (unsigned short)q01; qd[16] = (unsigned short)(q01 >> 16); qd[32] = (unsigned short)q23; qd[48] = (unsigned short)(q23 >> 16);
      LDS_DRAIN();
      FL[FL_Q] = 1u;
    } else {
      const unsigned short bias = ((LAS unsigned short*)(L + L_XR + 1024 + 768))[((j * 18) & 7) + min(lane, 17)];
      const int f = j * 18 + lane, lc = f >> 6;
      const int cnt = lane < 18 ? (576 * lc + 575) / 162 - (32 * lc) / 9 + 1 : 0;
      const int ha = WS_H + lc * 5 * 256 + (f & 15) * 16;
      pace<SETTLE>(tpub);
      lds_wait<2>(FL + FL_SLOT, NH);
      v4f hv;
#pragma nounroll
      for (int sp = 0; sp < SPIN_G; ++sp) {
        CBAR(); COUNT();
        v4u g[5];
#pragma unroll
        for (int k = 0; k < 5; ++k) g[k] = __builtin_amdgcn_raw_buffer_load_b128(rwh, k < cnt ? ha + k * 256 : OOB, 0, AUX_SC1);
        unsigned bad = 0;
        hv = v4f{0.f, 0.f, 0.f, 0.f};
#pragma unroll
        for (int k = 0; k < 5; ++k) hv += unseal(g[k], k < cnt ? tag : 0u, bad);
        if (__all((bad & TAG_MASK) == 0u)) break;
      }
      STAMP(4);
      const float hsum = (f & 32) ? ((f & 16) ? hv[3] : hv[2]) : ((f & 16) ? hv[1] : hv[0]);
      const float t = rbf(rbf(hsum) + asf((unsigned)bias << 16));
      const float z2 = 2.0f * 0.7978845608028654f * (t + 0.044715f * t * t * t);
      const float ge = t / (1.0f + __builtin_amdgcn_exp2f(-z2 * LOG2E));
      if (lane < 32) L[L_HS + lane] = lane < 18 ? cvtpk(ge, ge) : 0u;
      LDS_DRAIN();
      FL[FL_H] = 1u;
    }
    STAMP(5);
    VM_CLEAR();
  } else if (wave == W_C) {
    const unsigned tag32 = __builtin_amdgcn_raw_buffer_load_b32(rws, 0, 0, AUX_SC1) + 1u, tag = tag32 & TAG_MASK;
    lds_wait<1>(FL + FL_T0 + 1, 1u);
    unsigned t0 = __builtin_amdgcn_readfirstlane(FL[FL_T0]);
    const unsigned town = t0;
    auto mean_offset = [&](int off) {
      const v2u tt = __builtin_amdgcn_raw_buffer_load_b64(rws, WS_TT + off + c * 256, 0, AUX_SC1);
      const bool ok = tt.y == tag32;
      const float dsum = row_sum(ok ? (float)(int)(tt.x - town) : 0.f), dcnt = row_sum(ok ? 1.f : 0.f);
      float dv = ok ? (float)(int)(tt.x - town) : -1000.f;
      dv = fminf(dv, dpp<XOR1>(dv));
      const float dm = fmaxf(dsum * __builtin_amdgcn_rcpf(fmaxf(dcnt, 1.f)), row_max(dv) - (float)CAP);
      return __builtin_amdgcn_readfirstlane((int)fminf(fmaxf(dm, -150.f), 150.f));
    };
    if (s == 0) {
      lds_wait<1>(FL + FL_TS + 1, 1u);
      if (lane < 8) __builtin_amdgcn_raw_buffer_store_b64(v2u{FL[FL_TS], tag32}, rws, WS_TT + 128 + lane * 8, h * 256, AUX_SC1);
    }
    pace<TS>(t0);
    t0 = town + mean_offset(128);
    *(volatile LAS v2u*)(FL + FL_T1) = v2u{t0, 2u};
    STAMP(1);
    lds_wait<4>(FL + FL_FW, 1u);
    const int hh = lane >> 5;
    v4f fw = *(LAS v4f*)(L + L_FW + hh * 4);
    int oaddr = WS_O + j * O_STRIDE + (hh * 128 + c * 8 + (g4 & 1) * 4) * 4;
    const v2u mlp = *(LAS v2u*)(L + L_G + (lane & 1) * 2);
    v4u mls = seal(v4f{asf(mlp.x), asf(mlp.y), asf(mlp.x), asf(mlp.y)}, tag);
    int maddr = lane < 4 ? WS_O + j * O_STRIDE + 1024 + lane * 16 : OOB;
    PIN(fw); PIN(oaddr); PIN(mls); PIN(maddr);
    pace<slot_time(44)>(t0);
    STAMP(2);
    v4f o;
#pragma nounroll
    for (int sp = 0; sp < SPIN_L; ++sp) {
      CBAR();
      v4u d[4];
#pragma unroll
      for (int w = 0; w < 4; ++w) d[w] = *(LAS v4u*)(L + L_ORED + w * 256 + lane * 4);
      o = __builtin_bit_cast(v4f, d[0]) * fw[0];
#pragma unroll
      for (int w = 1; w < 4; ++w) o += __builtin_bit_cast(v4f, d[w]) * fw[w];
      const float chk = (o[0] + o[1]) + (o[2] + o[3]);
      if (__all(chk == chk)) break;
      if (__all(full(d[0]) & full(d[1]) & full(d[2]) & full(d[3]))) break;
    }
    STAMP(3);
    st128(seal(o, tag), rws, oaddr, 0);
    st128(mls, rws, maddr, 0);
    STAMP(4);
    pace<TO>(now());
    FL[FL_OPUB] = 1u;
    VM_CLEAR();
  } else {
    const int aw = wave - W_A;
    *(LAS v4u*)(L + L_ORED + aw * 256 + lane * 4) = v4u{EMPTY, EMPTY, EMPTY, EMPTY};
    if (aw != 0) {
      __builtin_amdgcn_s_sleep(TOUCH);
      if (aw == 1) { touch2(vc, kvo, kvo); touch2(out, 0, 0); }
      else touch2(aw == 2 ? wo : w2, j * (aw == 2 ? 18432 : 20736), j * (aw == 2 ? 18432 : 20736) + 18400);
    }
    const bool reducer = aw == 1 && (j & 31) < 9;
    const int hb = c >> 3;
    lds_wait<4>(FL + FL_PROG + aw, 2u);
    lds_wait<1>(FL + FL_PROG + aw, 3u);
    STAMP(1);
    v4u kf[16];
#pragma unroll
    for (int t = 0; t < 4; ++t)
#pragma unroll
      for (int kk = 0; kk < 4; ++kk)
        kf[t * 4 + kk] = *(LAS v4u*)(L + L_K + (aw * 16 + t * 4 + (c & 3)) * 256 + (c >> 2) * 64 + (((kk * 4 + g4) ^ c) * 4));
#pragma unroll
    for (int i = 0; i < 16; ++i) PIN(kf[i]);
    lds_wait<0>(FL + FL_Q, 1u);
    STAMP(2);
    v4u qf[4];
#pragma unroll
    for (int kk = 0; kk < 4; ++kk) qf[kk] = *(LAS v4u*)(L + L_QS + hb * 64 + (kk * 4 + g4) * 4);
    SB();
    v4f sc[4];
#pragma unroll
    for (int t = 0; t < 4; ++t) sc[t] = v4f{0.f, 0.f, 0.f, 0.f};
#pragma unroll
    for (int t = 0; t < 4; ++t)
#pragma unroll
      for (int kk = 0; kk < 4; ++kk) sc[t] = mfma(kf[t * 4 + kk], qf[kk], sc[t]);
    float sv[4];
#pragma unroll
    for (int e = 0; e < 4; ++e) {
      const float lo = (c & 1) ? sc[1][e] : sc[0][e], hi = (c & 1) ? sc[3][e] : sc[2][e];
      sv[e] = rbf(rbf((c & 2) ? hi : lo) * 0.08838834764831845f);
    }
    float m = fmaxf(fmaxf(sv[0], sv[1]), fmaxf(sv[2], sv[3]));
    m = fmaxf(m, dpp<XOR1>(m)); m = fmaxf(m, dpp<XOR2>(m));
    m = xrow_max(m);
    float p[4];
    v4u pk;
#pragma unroll
    for (int e = 0; e < 4; ++e) { p[e] = __builtin_amdgcn_exp2f((sv[e] - m) * LOG2E); pk[e] = cvtpk(p[e], p[e]); }
    *(LAS v4u*)(L + L_P + aw * 128 + (hb * 4 + g4) * 16 + (c & 3) * 4) = pk;
    float l = (p[0] + p[1]) + (p[2] + p[3]);
    l += dpp<XOR1>(l); l += dpp<XOR2>(l);
    l = xrow_sum(l);
    if ((lane & 55) == 0) *(LAS v2u*)(L + L_ML + aw * 4 + hb * 2) = v2u{asu(m), asu(l)};
    LDS_DRAIN();
    FL[FL_P + aw] = 1u;
    STAMP(3);
    const unsigned tag32 = L[L_XR + 512] + 1u, tag = tag32 & TAG_MASK;
    if (aw == 0) {
      lds_wait4<0>(FL + FL_P, 1u);
      const int hh = (lane >> 2) & 1, w = lane & 3;
      const v2u ml = *(LAS v2u*)(L + L_ML + w * 4 + hh * 2);
      float M = asf(ml.x);
      M = fmaxf(M, dpp<XOR1>(M)); M = fmaxf(M, dpp<XOR2>(M));
      const float fw = __builtin_amdgcn_exp2f((asf(ml.x) - M) * LOG2E);
      float Ls = asf(ml.y) * fw;
      Ls += dpp<XOR1>(Ls); Ls += dpp<XOR2>(Ls);
      if (lane < 8) L[L_FW + lane] = asu(fw);
      if ((lane & 59) == 0) *(LAS v2u*)(L + L_G + hh * 2) = v2u{asu(M), asu(Ls)};
      LDS_DRAIN();
      FL[FL_FW] = 1u;
      STAMP(4);
    } else if (reducer) {
      const int red = (j >> 5) * 9 + (j & 31);
      const int ocol = red * 8 + (lane & 1) * 4 + g4;
      const unsigned xb = ((LAS unsigned short*)(L + L_XR))[ocol];
      const unsigned bb = ((LAS unsigned short*)(L + L_XR + 768))[ocol & 7];
      float base = asf(xb << 16) + asf(bb << 16);
      PIN(base);
      lds_wait<4>(FL + FL_OPUB, 1u);
      lds_wait<0>(FL + FL_FPUB, 5u);
      pace<TF>(now());
      STAMP(4);
      v4f sum;
      bool done = false;
#pragma nounroll
      for (int sp = 0; sp < SPIN_G; ++sp) {
        CBAR(); COUNT();
        if (!done) {
          v4u pv[8];
#pragma unroll
          for (int i = 0; i < 8; ++i) pv[i] = __builtin_amdgcn_raw_buffer_load_b128(rws, (lane >> 1) * 64 + (lane & 1) * 16 + (i & 1) * 2048, WS_F + (red >> 1) * F_STRIDE + (red & 1) * 32 + (i >> 1) * 4096, AUX_SC1);
          unsigned bad = 0;
          sum = unseal(pv[0], tag, bad);
#pragma unroll
          for (int i = 1; i < 8; ++i) sum += unseal(pv[i], tag, bad);
          done = (bad & TAG_MASK) == 0u;
        }
        if (__all(done)) break;
      }
      STAMP(5);
      auto s02 = __builtin_amdgcn_permlane32_swap(asu(sum[0]), asu(sum[2]), false, false);
      auto s13 = __builtin_amdgcn_permlane32_swap(asu(sum[1]), asu(sum[3]), false, false);
      const float u0 = asf(s02[0]) + asf(s02[1]), u1 = asf(s13[0]) + asf(s13[1]);
      auto s01 = __builtin_amdgcn_permlane16_swap(asu(u0), asu(u1), false, false);
      float u = asf(s01[0]) + asf(s01[1]);
      u += dpp<ROR8>(u); u += dpp<ROR4>(u); u += dpp<XOR2>(u);
      u += base;
      if ((lane & 14) == 0) __builtin_amdgcn_raw_buffer_store_b16((unsigned short)cvtpk(u, u), RS(out, D * 2), ocol * 2, 0, 0);
      if (j == 0 && lane == 0) __builtin_amdgcn_raw_buffer_store_b32(tag32, rws, 0, 0, AUX_SC1);
      STAMP(6);
    }
    VM_CLEAR();
  }

  if ((wave < 4) && t_ev != 0) { for (int sp = 0; sp < 100000 && now() - t_ev < 4000u; ++sp) __builtin_amdgcn_s_sleep(2); }
}

extern "C" __device__ __attribute__((used, noinline)) void fused_decode_end() {}

extern "C" void kernel_launch(void* const* d_in, const int* in_sizes, int n_in, void* d_out, int out_size, void* d_ws, size_t ws_size,
                              hipStream_t stream) {
  fused_decode<<<256, NWAVE * 64, 0, stream>>>(d_in[3], d_in[1], d_in[6], d_in[2], d_in[4], d_in[8], d_ws, d_in[0], d_in[7], d_in[9], d_out);
}
```

```cpp
#include <hip/hip_runtime.h>
#include <stdint.h>
#include <type_traits>

typedef unsigned v4u __attribute__((ext_vector_type(4)));
typedef unsigned v2u __attribute__((ext_vector_type(2)));
typedef float v4f __attribute__((ext_vector_type(4)));
typedef float v2f __attribute__((ext_vector_type(2)));
typedef short v4s __attribute__((ext_vector_type(4)));
typedef __bf16 bf16x8 __attribute__((ext_vector_type(8)));
typedef __bf16 bf16x2 __attribute__((ext_vector_type(2)));
#define LAS __attribute__((address_space(3)))

constexpr int D = 576, SK = 16384, WIN = 4096, F = 4608;
constexpr int KV_BYTES = 16 * SK * 128 * 2;
constexpr int WQ_BYTES = D * 4096 * 2, W1_BYTES = D * F * 2;
constexpr int WS_TT = 4096  , WS_TB = 8192  , WS_Q = 16384, WS_H = WS_Q + 256 * 256, WS_O = WS_H + 72 * 5 * 256, O_STRIDE = 1152,
              WS_F = WS_O + 256 * O_STRIDE, F_STRIDE = 256 * 64 + 256, WS_END = WS_F + 36 * F_STRIDE;
constexpr unsigned EMPTY = 0xFFFFFFFFu;
constexpr int OOB = 0x7FFFFFFF;
#define WINDOW 11
#define PACE10 148
#define PACE0 80
#define BLIND 3
#define AHEAD 6
#define LAG 3
#define LAG1 4
#define LAG2 7
#define LAG3 5
#define TQ (slot_time(27))
#define TH (slot_time(31))
#define TM (slot_time(48))
#define EARLY AHEAD
#define DRIFT0 (SMP + 19)
#define TS (slot_time(SMP + 13))
#define SYNC 1
#define OATOM 0
#define TOUCH 60
#define EXTRA 0
#define STAG 0
#define PV0 42
#define TG 15
#define F4S 43
#define F4D 1
#define FCOST 92
#define SMP 12
#define RTT0 55
#define HAT 47
#define WE 11
#define WEN 28
#define NQ 27
#define NH 33
#define TA (slot_time(9))
#define GAP 200
#define KVORD 1
#define CAP 35
#define OG 49
#define BEACON 0
#define BIAS 35
#define LEAD 0
#define DRIFTA 12
#define SETTLE 60
#define TO 35
#define TF 25
constexpr int AUX_NT = 2, AUX_SC1 = 16, W = WINDOW;
#define ABL 0
constexpr int SPIN_G = ABL ? 1 : 20000, SPIN_L = 400000;
constexpr float LOG2E = 1.4426950408889634f;

constexpr int L_WQ = 0  , L_K = 5120  , L_W1 = L_K + 16384  , L_FLAG = L_W1 + 6144  ;
constexpr int FL_Q = 0, FL_H = 1, FL_PROG = 4  , FL_P = 8  , FL_KDONE = 13, FL_FW = 14, FL_G = 15, FL_VDONE = 16, FL_OPUB = 17,
              FL_FPUB = 18, FL_T0 = 20  , FL_T1 = 22  , FL_TS = 24  , FL_SLOT = 26;
constexpr int L_XR = L_FLAG + 32  , L_Y = L_XR + 2 * 1024  , L_QS = L_Y + 2 * 512  , L_HS = L_QS + 128  ,
              L_ML = L_HS + 32  , L_FW = L_ML + 16  , L_G = L_FW + 16  , L_OS = L_G + 16  , L_ORED = L_OS + 16  ,
              L_P = L_ORED + 1280  , L_END = L_P + 512;
constexpr int YZ = 192;

#define RS(p, n) __builtin_amdgcn_make_buffer_rsrc((void*)(p), 0, (int)(n), 0x00020000)
#define SB() __builtin_amdgcn_sched_barrier(0)
#define CBAR() asm volatile("" ::: "memory")
#define LDS_DRAIN() asm volatile("s_waitcnt lgkmcnt(0)" ::: "memory")
#define VM_DRAIN() asm volatile("s_waitcnt vmcnt(0)" ::: "memory")
#define VM_CLEAR() __builtin_amdgcn_s_waitcnt(0x0F70)
#define PIN(x) asm volatile("" : "+v"(x))

#define COUNT() do {} while (0)
#define STAMP(i) do {} while (0)

__device__ __forceinline__ float asf(unsigned u) { return __builtin_bit_cast(float, u); }
__device__ __forceinline__ unsigned asu(float f) { return __builtin_bit_cast(unsigned, f); }
__device__ __forceinline__ unsigned cvtpk(float lo, float hi) {
  return __builtin_bit_cast(unsigned, __builtin_convertvector(v2f{lo, hi}, bf16x2));
}
__device__ __forceinline__ v2f unpk(unsigned p) { return v2f{asf(p << 16), asf(p & 0xFFFF0000u)}; }
__device__ __forceinline__ float rbf(float f) { return asf(cvtpk(0.f, f)); }
__device__ __forceinline__ float vmax(float a, float b) { float r; asm("v_max_f32 %0, %1, %2" : "=v"(r) : "v"(a), "v"(b)); return r; }
__device__ __forceinline__ float vmax3(float a, float b, float c) { float r; asm("v_max3_f32 %0, %1, %2, %3" : "=v"(r) : "v"(a), "v"(b), "v"(c)); return r; }
template <int CTRL> __device__ __forceinline__ float dpp(float x) {
  return asf(__builtin_amdgcn_mov_dpp(asu(x), CTRL, 0xf, 0xf, true));
}
constexpr int XOR1 = 0xB1, XOR2 = 0x4E, XOR7 = 0x141, ROR8 = 0x128, ROR4 = 0x124;
__device__ __forceinline__ float xrow_sum(float x) {
  auto s = __builtin_amdgcn_permlane16_swap(asu(x), asu(x), false, false);
  x = asf(s[0]) + asf(s[1]);
  auto t = __builtin_amdgcn_permlane32_swap(asu(x), asu(x), false, false);
  return asf(t[0]) + asf(t[1]);
}
__device__ __forceinline__ float xrow_max(float x) {
  auto s = __builtin_amdgcn_permlane16_swap(asu(x), asu(x), false, false);
  x = fmaxf(asf(s[0]), asf(s[1]));
  auto t = __builtin_amdgcn_permlane32_swap(asu(x), asu(x), false, false);
  return fmaxf(asf(t[0]), asf(t[1]));
}
__device__ __forceinline__ float row_sum(float x) {
  x += dpp<XOR1>(x); x += dpp<XOR2>(x); x += dpp<XOR7>(x); x += dpp<ROR8>(x);
  return x;
}
__device__ __forceinline__ float row_max(float x) {
  asm("s_nop 1\n\tv_max_f32_dpp %0, %0, %0 quad_perm:[1,0,3,2] row_mask:0xf bank_mask:0xf\n\ts_nop 1\n\t"
      "v_max_f32_dpp %0, %0, %0 quad_perm:[2,3,0,1] row_mask:0xf bank_mask:0xf\n\ts_nop 1\n\t"
      "v_max_f32_dpp %0, %0, %0 row_half_mirror row_mask:0xf bank_mask:0xf\n\ts_nop 1\n\t"
      "v_max_f32_dpp %0, %0, %0 row_ror:8 row_mask:0xf bank_mask:0xf" : "+v"(x));
  return x;
}
__device__ __forceinline__ v4f mfma(v4u a, v4u b, v4f c) {
  return __builtin_amdgcn_mfma_f32_16x16x32_bf16(__builtin_bit_cast(bf16x8, a), __builtin_bit_cast(bf16x8, b), c, 0, 0, 0);
}
__device__ __forceinline__ v4u box(v4f v) {
  v4u r;
#pragma unroll
  for (int i = 0; i < 4; ++i) r[i] = min(asu(v[i]), 0xFFFFFFFEu);
  return r;
}
constexpr unsigned TAG_MASK = 0x3FFu;
__device__ __forceinline__ v4u seal(v4f v, unsigned tag) {
  v4u r;
#pragma unroll
  for (int i = 0; i < 4; ++i) r[i] = (asu(v[i]) & ~TAG_MASK) | tag;
  return r;
}
__device__ __forceinline__ v4f unseal(v4u w, unsigned tag, unsigned& bad) {
  const v4u x = w ^ tag;
  bad |= (x.x | x.y) | (x.z | x.w);
  return __builtin_bit_cast(v4f, x);
}
__device__ __forceinline__ bool full(v4u v) { return max(max(v.x, v.y), max(v.z, v.w)) != EMPTY; }
template <int NAP = 2> __device__ __forceinline__ void lds_wait(volatile LAS unsigned* f, unsigned need) {
#pragma nounroll
  for (int sp = 0; sp < SPIN_L; ++sp) {
    if ((unsigned)__builtin_amdgcn_readfirstlane(*f) >= need) break;
    if (NAP) __builtin_amdgcn_s_sleep(NAP);
  }
  CBAR();
}
template <int NAP> __device__ __forceinline__ void lds_wait4(volatile LAS unsigned* f, unsigned need) {
#pragma nounroll
  for (int sp = 0; sp < SPIN_L; ++sp) {
    const v4u v = *(volatile LAS v4u*)f;
    if ((unsigned)__builtin_amdgcn_readfirstlane(min(min(v.x, v.y), min(v.z, v.w))) >= need) break;
    if (NAP) __builtin_amdgcn_s_sleep(NAP);
  }
  CBAR();
}
__device__ __forceinline__ void lds_inc(LAS unsigned* f, int lane) {
  if (lane == 0) __hip_atomic_fetch_add(f, 1u, __ATOMIC_RELAXED, __HIP_MEMORY_SCOPE_WORKGROUP);
}
typedef __attribute__((address_space(1))) unsigned long long gu64;
__device__ __forceinline__ v2u peek(void* ws, int off) {
  unsigned long long zero = 0ull;
  asm volatile("" : "+v"(zero));
  const unsigned long long r = __hip_atomic_fetch_or((gu64*)((char*)ws + off), zero, __ATOMIC_RELAXED, __HIP_MEMORY_SCOPE_AGENT);
  return v2u{(unsigned)r, (unsigned)(r >> 32)};
}
__device__ __forceinline__ void touch_and_barrier(const void* p, unsigned o0, unsigned o1, unsigned o2, const void* q, unsigned o3) {
  unsigned d0, d1, d2, d3;
  asm volatile("s_load_dword %0, %4, %5\n\ts_load_dword %1, %4, %6\n\ts_load_dword %2, %4, %7\n\ts_load_dword %3, %8, %9\n\ts_barrier\n\ts_waitcnt lgkmcnt(0)"
               : "=&s"(d0), "=&s"(d1), "=&s"(d2), "=&s"(d3) : "s"(p), "s"(o0), "s"(o1), "s"(o2), "s"(q), "s"(o3) : "memory");
}
__device__ __forceinline__ void touch2(const void* p, unsigned o0, unsigned o1) {
  unsigned d0, d1;
  asm volatile("s_load_dword %0, %2, %3\n\ts_load_dword %1, %2, %4\n\ts_waitcnt lgkmcnt(0)" : "=&s"(d0), "=&s"(d1) : "s"(p), "s"(o0), "s"(o1) : "memory");
}
__device__ __forceinline__ void poke(void* ws, int off, unsigned lo, unsigned hi) {
  (void)__hip_atomic_exchange((gu64*)((char*)ws + off), ((unsigned long long)hi << 32) | lo, __ATOMIC_RELAXED, __HIP_MEMORY_SCOPE_AGENT);
}
__device__ __forceinline__ unsigned now() {
  unsigned long long t;
  asm volatile("s_memrealtime %0\n\ts_waitcnt lgkmcnt(0)" : "=s"(t)::"memory");
  return (unsigned)t;
}
template <int T> __device__ __forceinline__ void pace(unsigned t0) {
  asm volatile("1:\n\ts_memrealtime s[90:91]\n\ts_waitcnt lgkmcnt(0)\n\ts_sub_u32 s90, s90, %0\n\ts_cmpk_lt_u32 s90, %1\n\ts_cbranch_scc1 1b"
               ::"s"(__builtin_amdgcn_readfirstlane(t0)), "i"(T) : "memory", "scc", "s90", "s91");
}
constexpr int slot_cost(int n) { return n == 10 ? 30 : n == 42 ? GAP : (n >= 43 ? (n == 47 ? 60 : FCOST) : 100); }
constexpr int slot_time(int n) {
  if (n < AHEAD) return (n - BLIND + 1) * 15;
  int c = 0;
  for (int i = AHEAD; i < n; ++i) c += slot_cost(i);
  return PACE0 + c * PACE10 / 1000;
}
constexpr int pv_done(int n) { return n < PV0 ? 0 : ((n - PV0) * 5 + 2 < n - 26 - LAG ? (n - PV0) * 5 + 2 : n - 26 - LAG); }
template <int N> __device__ __forceinline__ void vmwait() { asm volatile("s_waitcnt vmcnt(%0)" ::"i"(N) : "memory"); }
__device__ __forceinline__ void st128(v4u d, __amdgpu_buffer_rsrc_t r, int voff, int soff) {
  __builtin_amdgcn_raw_buffer_store_b128(d, r, voff, soff, AUX_SC1);
  asm volatile("s_nop 1" ::"v"(d) : "memory");
}
__device__ __forceinline__ void dma(__amdgpu_buffer_rsrc_t r, int voff, int soff, int ldsaddr) {
  asm volatile("s_mov_b32 m0, %0\n\ts_nop 0\n\tbuffer_load_dwordx4 %1, %2, %3 offen nt lds" ::"s"(ldsaddr), "v"(voff), "s"(r), "s"(soff) : "memory");
}
__device__ __forceinline__ void dma_dev(__amdgpu_buffer_rsrc_t r, int voff, int ldsaddr) {
  asm volatile("s_mov_b32 m0, %0\n\ts_nop 0\n\tbuffer_load_dwordx4 %1, %2, 0 offen sc1 lds" ::"s"(ldsaddr), "v"(voff), "s"(r) : "memory");
}
__device__ __forceinline__ v4u keep_mask(int m) {
  v4u k;
#pragma unroll
  for (int v = 0; v < 4; ++v) k[v] = ((m >> 1) == v) ? ((m & 1) ? 0xFFFF0000u : 0x0000FFFFu) : 0u;
  return k;
}
template <int I, int N, class Fn> __device__ __forceinline__ void static_for(Fn&& fn) {
  if constexpr (I < N) { fn(std::integral_constant<int, I>{}); static_for<I + 1, N>(fn); }
}
__device__ __forceinline__ void layernorm(unsigned (&p)[5], int lane) {
  const float c0 = asf(__builtin_amdgcn_readfirstlane(p[0]) << 16);
  v2f v[5], d[5];
#pragma unroll
  for (int i = 0; i < 5; ++i) { v[i] = unpk(p[i]); d[i] = v[i] - c0; }
  if (lane >= 32) d[4] = v2f{0.f, 0.f};
  const v2f s1v = (d[0] + d[1]) + (d[2] + d[3]) + d[4];
  v2f s2v = d[0] * d[0];
#pragma unroll
  for (int i = 1; i < 5; ++i) s2v = __builtin_elementwise_fma(d[i], d[i], s2v);
  const float s1 = xrow_sum(row_sum(s1v.x + s1v.y)) * (1.0f / D), s2 = xrow_sum(row_sum(s2v.x + s2v.y)) * (1.0f / D);
  const float var = fmaxf(fmaf(-s1, s1, s2), 0.f);
  const float mb = rbf(c0 + s1), sd = rbf(__builtin_amdgcn_sqrtf(rbf(rbf(var) + 1e-5f)));
  const float inv = __builtin_amdgcn_rcpf(sd);
#pragma unroll
  for (int i = 0; i < 5; ++i) {
    const v2f t = v[i] - mb;
    const v2f u = v2f{rbf(t.x), rbf(t.y)} * inv;
    p[i] = cvtpk(u.x, u.y);
  }
}

constexpr int W_F4 = 4, W_C = 5, W_P = 6, W_A = 8, NWAVE = 12;

extern "C" __global__ void __launch_bounds__(NWAVE * 64, 3)
fused_decode(const void* __restrict__ wq, const void* __restrict__ kc, const void* __restrict__ w1, const void* __restrict__ vc,
             const void* __restrict__ wo, const void* __restrict__ w2, void* __restrict__ ws, const void* __restrict__ x,
             const void* __restrict__ b1, const void* __restrict__ b2, void* __restrict__ out) {
  __shared__ __attribute__((aligned(16))) unsigned smem[L_END];
  LAS unsigned* L = (LAS unsigned*)smem;
  volatile LAS unsigned* FL = (volatile LAS unsigned*)smem + L_FLAG;
  const int tid = threadIdx.x, lane = tid & 63, wave = __builtin_amdgcn_readfirstlane(tid >> 6);
  const int b = blockIdx.x, j = (b & 7) * 32 + (b >> 3), h = j >> 4, s = j & 15;
  const int r8 = lane >> 3, c8 = lane & 7, g4 = lane >> 4, c = lane & 15;
  const int lcA = (j * 9) >> 5, rowA = 18 * ((j * 9) & 31);
  const int lds0 = (int)(unsigned)(uintptr_t)L;
  if (!(ABL & 64) && (wave >> 1) == (W_P >> 1)) {
    const int dst = lds0 + (L_XR + (wave & 1) * 1024) * 4;
    dma_dev(RS(x, D * 2), lane * 16, dst);
    dma_dev(RS(x, D * 2), lane * 16 + 1024, dst + 1024);
    dma_dev(RS(ws, 16), lane * 16, dst + 2048);
    const int red8 = ((j >> 5) * 9 + min(j & 31, 8)) * 16;
    dma_dev(RS((wave & 1) ? (const char*)b1 : (const char*)b2, (wave & 1) ? F * 2 : D * 2), lane < 4 ? lane * 16 + ((wave & 1) ? (j * 36) & ~15 : red8) : OOB, dst + 3072);
  }
  if (!(ABL & 128) && wave == W_C) {
    unsigned long long pc;
    unsigned pcoff, len;
    asm volatile("s_getpc_b64 %0\n.Lpc%=:\n\ts_mov_b32 %1, .Lpc%=-fused_decode\n\ts_mov_b32 %2, fused_decode_end-fused_decode"
                 : "=s"(pc), "=s"(pcoff), "=s"(len));
    dma_dev(RS(pc - pcoff, len), lane < 40 ? lane * 16 + (b >> 3) * 640 : OOB, lds0 + (L_ORED + 1024) * 4);
  }
  const unsigned kvo = (h * SK + (SK - WIN) + s * 16) * 256;
  if (wave == W_A) {
    const unsigned c1 = lcA * 128;
    touch_and_barrier(w1, rowA * 9216 + c1, min(rowA + 161, D - 1) * 9216 + c1, c1, kc, kvo);
  } else if (wave >= 4) {
    if (wave == W_F4 && lane < 32) L[L_FLAG + lane] = 0u;
    LDS_DRAIN();
    __builtin_amdgcn_s_barrier();
    CBAR();
  }
  const auto rws = RS(ws, WS_END);
  const auto rwq = RS(ws, (ABL & 4) ? 0 : WS_END), rwh = RS(ws, (ABL & 8) ? 0 : WS_END), rwm = RS(ws, (ABL & 16) ? 0 : WS_END), rwp = RS(ws, (ABL & 32) ? 0 : WS_END);

  if (__builtin_expect(wave <= W_F4, 1)) {
    v4u wf[9], ha[5], mskf, mw, ow;
    int vo, v2, faddr, oga, mga;
    unsigned tag;
    auto prep_addr = [&]() {
      const bool live = wave < 4 || c < 8;
      vo = live ? g4 * 4608 + wave * 256 + c * 16 : OOB - 4096;
      v2 = live ? g4 * 1152 + wave * 256 + c * 16 : OOB - 5 * 4608;
      faddr = lane < 32 && live ? WS_F + (wave * 8 + (c >> 1)) * F_STRIDE + j * 64 + (c & 1) * 32 + g4 * 16 : OOB;
      oga = WS_O + (h * 16 + c) * O_STRIDE + s * 64 + g4 * 16;
      mga = WS_O + 1024 + (h * 16 + c) * O_STRIDE + (s >> 3) * 16;
      mskf = keep_mask(c);
      PIN(mga); PIN(vo); PIN(v2); PIN(faddr); PIN(oga); PIN(mskf);
    };
    auto prep_h = [&]() {
      lds_wait<0>(FL + FL_H, 1u);
      unsigned hv[5];
      const unsigned ep = L[L_XR + 512];
#pragma unroll
      for (int rg = 0; rg < 5; ++rg) hv[rg] = L[L_HS + rg * 4 + g4];
      SB();
      tag = (ep + 1u) & TAG_MASK;
#pragma unroll
      for (int rg = 0; rg < 5; ++rg) ha[rg] = mskf & hv[rg];
      asm volatile("" : "+v"(tag), "+v"(ha[0]), "+v"(ha[1]), "+v"(ha[2]), "+v"(ha[3]), "+v"(ha[4]));
    };
    if (__builtin_expect(wave < 4, 1)) {
      const auto rq = RS(wq, WQ_BYTES), rk = RS(kc, KV_BYTES), rv = RS(vc, KV_BYTES), r1 = RS(w1, W1_BYTES);
      const int qs = (s >> 2) * 144 * 8192 + h * 512 + (s & 3) * 128;
      const int kvb = (h * SK + (SK - WIN) + s * 16) * 256 + wave * 1024, rot = h;
      const int cs = (c8 ^ (r8 & 6)) * 16;
      __amdgpu_buffer_rsrc_t ro, r2;
      v4u vv[16], pk[4], mskv, alast;
      v4f acc = {0.f, 0.f, 0.f, 0.f};
      unsigned t0;
      int dacc = 0, dstep = 0;
      static_for<0, 52>([&](auto nc) {
        constexpr int n = nc;
        if (n == 1) t0 = now();
        if (n == BLIND) __builtin_amdgcn_s_barrier();
        if (n == AHEAD) {
          vmwait<AHEAD - 1>();
          const unsigned t1 = now() - slot_time(n);
          if ((int)(t1 - t0) > 0) t0 = t1 + min((int)(t1 - t0), EXTRA);
          if (wave == 0) *(volatile LAS v2u*)(FL + FL_T0) = v2u{t0, 1u};
        }
        if (n < WEN) { if (n >= WE) vmwait<WE - 1>(); } else vmwait<W - 1>();
        if constexpr (n >= DRIFTA && n < 43) dacc += dstep;
        if constexpr (n >= DRIFTA) pace<slot_time(n)>(t0 + ((dacc + 128) >> 8));
        else if constexpr (n >= BLIND) pace<slot_time(n)>(t0);
        if constexpr (n < 5) {
          dma(rq, min((n * 4 + wave) * 8 + r8, 143) * 8192 + cs, qs, lds0 + L_WQ * 4 + (n * 4 + wave) * 1024);
        } else if constexpr (n < 11) {
          constexpr int i = n - 5;
          const unsigned v = (unsigned)((rowA + min((i * 4 + wave) * 8 + r8, 161)) * 9216 + cs);
          dma(r1, (int)min(v, v - (unsigned)(W1_BYTES - 128)), lcA * 128, lds0 + L_W1 * 4 + (i * 4 + wave) * 1024);
        } else if constexpr (n < 27) {
          constexpr int i = n - 11;
          dma(rk, g4 * 256 + ((c ^ (g4 * 4 + (i & 3))) * 16), kvb + (((KVORD ? (i & 3) * 4 + (i >> 2) : i) + rot) & 15) * 65536, lds0 + L_K * 4 + (wave * 16 + i) * 1024);
        } else if constexpr (n < 43) {
          constexpr int i = n - 27;
          vv[i] = __builtin_amdgcn_raw_buffer_load_b128(rv, lane * 16, kvb + (((KVORD ? (i & 3) * 4 + (i >> 2) : i) + rot) & 15) * 65536, AUX_NT);
        } else if constexpr (n < 48) {
          wf[n - 43] = __builtin_amdgcn_raw_buffer_load_b128(r2, v2 + (n - 43) * 4608, 0, AUX_NT);
        } else {
          wf[n - 43] = __builtin_amdgcn_raw_buffer_load_b128(ro, vo, (n - 48) * 1152, AUX_NT);
        }
        SB();
        if (n == AHEAD) STAMP(7);
        if (n == 4 + LAG1) { vmwait<LAG1>(); FL[FL_PROG + wave] = 1u; STAMP(1); }
        if (n == 10 + LAG2) { vmwait<LAG2>(); FL[FL_PROG + wave] = 2u; }
        if (n == 26 + LAG3) { vmwait<LAG3>(); FL[FL_PROG + wave] = 3u; STAMP(2); }
        if (n == SMP + 3 && wave == 0) {
          if (s == 0) {
            vmwait<3>();
            const unsigned t1 = now() - (slot_time(SMP) + RTT0);
            *(volatile LAS v2u*)(FL + FL_TS) = v2u{(int)(t1 - t0) > 0 ? t1 : t0, 1u};
          }
        }
        if (n == 21) prep_addr();
        if (n == 22) {
          mskv = keep_mask(c8);
          PIN(mskv);
          ro = RS((const char*)wo + j * 18432, 18432);
          r2 = RS((const char*)w2 + j * 20736, 20736);
          asm volatile("" : "+s"(ro), "+s"(r2));
        }
        if ((n == NQ || n == NH) && wave == 0) FL[FL_SLOT] = n;
        if (n == DRIFTA - 1 || n == DRIFTA + 2 || n == DRIFT0 - 1 || n == DRIFT0 + 2 || n == 37) {
          const v2u t1 = *(volatile LAS v2u*)(FL + FL_T1);
          if (__builtin_amdgcn_readfirstlane(t1.y)) dstep = (((int)(__builtin_amdgcn_readfirstlane(t1.x) - t0) * 256 - dacc) * (4096 / (n < 42 ? 42 - n : 1))) >> 12;
        }
        if (n == PV0) {
          lds_wait<0>(FL + FL_P + wave, 1u);
          STAMP(3);
#pragma unroll
          for (int t = 0; t < 4; ++t) pk[t] = *(LAS v4u*)(L + L_P + wave * 128 + ((c >> 3) * 4 + g4) * 16 + t * 4);
          alast = mskv & pk[3][3];
          PIN(alast);
        }
        if constexpr (n >= PV0 && n <= 42 + LAG) {
          constexpr int done = pv_done(n - 1), ready = pv_done(n);
#pragma unroll
          for (int i = done; i < ready; ++i) acc = mfma(i == 15 ? alast : mskv & pk[i >> 2][i & 3], vv[i], acc);
          if (n == 42 + LAG) {
            *(LAS v4u*)(L + L_ORED + wave * 256 + lane * 4) = box(acc);
            STAMP(4);
          }
        }
        if (n == HAT) prep_h();
        if (n == OG) {
          lds_wait<0>(FL + FL_OPUB, 1u);
          STAMP(5);
          mw = __builtin_amdgcn_raw_buffer_load_b128(rws, mga, 0, AUX_SC1);
          ow = __builtin_amdgcn_raw_buffer_load_b128(rws, oga, 0, AUX_SC1);
        }
        SB();
      });
    } else {
      prep_addr();
      const auto ro = RS((const char*)wo + j * 18432, 18432), r2 = RS((const char*)w2 + j * 20736, 20736);
      lds_wait<4>(FL + FL_T1 + 1, 2u);
      const unsigned t0 = __builtin_amdgcn_readfirstlane(FL[FL_T1]);
      static_for<0, 9>([&](auto nc) {
        constexpr int n = nc;
        pace<slot_time(F4S + n * F4D)>(t0);
        if constexpr (n < 5) asm volatile("buffer_load_dwordx4 %0, %1, %2, 0 offen nt" : "=v"(wf[n]) : "v"(v2 + n * 4608), "s"(r2) : "memory");
        else asm volatile("buffer_load_dwordx4 %0, %1, %2, 0 offen offset:%3 nt" : "=v"(wf[n]) : "v"(vo), "s"(ro), "i"((n - 5) * 1152) : "memory");
        SB();
        if (n + 43 == HAT) prep_h();
        if (n + 43 == OG) {
          lds_wait<0>(FL + FL_OPUB, 1u);
          STAMP(5);
          asm volatile("buffer_load_dwordx4 %0, %2, %4, 0 offen sc1\n\tbuffer_load_dwordx4 %1, %3, %4, 0 offen sc1"
                       : "=&v"(mw), "=&v"(ow) : "v"(mga), "v"(oga), "s"(rws) : "memory");
        }
        SB();
      });
    }
    v4f a2 = {0.f, 0.f, 0.f, 0.f};
#pragma unroll
    for (int rg = 0; rg < 5; ++rg) a2 = mfma(ha[rg], wf[rg], a2);
    PIN(a2);
    SB();
    unsigned bad = 0;
    v4f ml = unseal(mw, tag, bad), u = unseal(ow, tag, bad);
    COUNT();
    if (__builtin_expect(!__all((bad & TAG_MASK) == 0u), 0)) {
#pragma nounroll
      for (int sp = 0; sp < SPIN_G; ++sp) {
        COUNT();
        CBAR();
        bad = 0;
        ml = unseal(__builtin_amdgcn_raw_buffer_load_b128(rws, mga, 0, AUX_SC1), tag, bad);
        u = unseal(__builtin_amdgcn_raw_buffer_load_b128(rws, oga, 0, AUX_SC1), tag, bad);
        if (__all((bad & TAG_MASK) == 0u)) break;
      }
    }
    STAMP(6);
    const float gw = __builtin_amdgcn_exp2f((ml[0] - row_max(ml[0])) * LOG2E);
    float r[5] = {u[0] * gw, u[1] * gw, u[2] * gw, u[3] * gw, ml[1] * gw};
#pragma unroll
    for (int i = 4; i >= 0; --i) r[i] += dpp<XOR1>(r[i]);
#pragma unroll
    for (int i = 4; i >= 0; --i) r[i] += dpp<XOR2>(r[i]);
#pragma unroll
    for (int i = 4; i >= 0; --i) r[i] += dpp<XOR7>(r[i]);
#pragma unroll
    for (int i = 4; i >= 0; --i) r[i] += dpp<ROR8>(r[i]);
    const float inv = __builtin_amdgcn_rcpf(r[4]);
    v4u oa[4];
#pragma unroll
    for (int rg = 0; rg < 4; ++rg) oa[rg] = mskf & cvtpk(r[rg] * inv, r[rg] * inv);
    SB();
#pragma unroll
    for (int rg = 0; rg < 4; ++rg) a2 = mfma(oa[rg], wf[5 + rg], a2);
    st128(seal(a2, tag), rws, faddr, 0);
    lds_inc(L + L_FLAG + FL_FPUB, lane);
    if (wave != 3) STAMP(7);
  } else if ((wave >> 1) == (W_P >> 1)) {
    const int pw = wave - W_P;
    LAS unsigned* Y = L + L_Y + pw * 512;
    *(LAS v4u*)(Y + lane * 8) = v4u{0u, 0u, 0u, 0u};
    *(LAS v4u*)(Y + lane * 8 + 4) = v4u{0u, 0u, 0u, 0u};
    const int nA = min(162, D - rowA), sh = pw == 0 ? 0 : (rowA & 2);
    const int ya = YZ + sh + (pw == 0 ? (s >> 2) * 144 : rowA), yb = pw == 0 ? ya : (nA == 162 ? 0 : YZ + sh - nA);
    const LAS char* ap = (const LAS char*)Y + ((c < 8 ? ya : yb) + g4 * 4) * 2;
    const LAS char* bp = (const LAS char*)(L + (pw == 0 ? L_WQ : L_W1)) + (g4 * 4 + (c >> 2)) * 128 + (c & 3) * 8;
    const int key = (g4 & 1) * 2 + (c >> 3);
    const int gA = pw == 0 ? WS_Q + j * 256 : WS_H + (lcA * 5 + j - (32 * lcA) / 9) * 256;
    int gout = g4 == 0 ? gA + c * 16 : (g4 == 2 && pw == 1 && nA < 162 ? WS_H + (lcA + 1) * 5 * 256 + c * 16 : OOB);
    PIN(gout);
    VM_DRAIN();
    STAMP(1);
    unsigned xp[5];
#pragma unroll
    for (int i = 0; i < 5; ++i) xp[i] = L[L_XR + pw * 1024 + i * 64 + lane];
    const unsigned tag = (L[L_XR + pw * 1024 + 512] + 1u) & TAG_MASK;
#pragma nounroll
    for (int rep = 0; rep <= pw; ++rep) layernorm(xp, lane);
#pragma unroll
    for (int i = 0; i < 5; ++i)
      if (i < 4 || lane < 32) Y[(YZ + sh) / 2 + i * 64 + lane] = xp[i];
    v4u af[6];
#pragma unroll
    for (int ks = 0; ks < 6; ++ks) {
      const v2u lo = *(const LAS v2u*)(ap + ks * 64), hi = *(const LAS v2u*)(ap + ks * 64 + 32);
      af[ks] = v4u{lo.x, lo.y, hi.x, hi.y};
    }
    if (pw == 0) { af[4].z = 0u; af[4].w = 0u; }
    af[5] = v4u{g4 == 0 ? af[5].x : 0u, 0u, 0u, 0u};
#pragma unroll
    for (int ks = 0; ks < 6; ++ks) PIN(af[ks]);
    STAMP(2);
    lds_wait4<1>(FL + FL_PROG, pw + 1u);
    v4f acc[4];
    {
      v4u bf[5][4];
#pragma unroll
      for (int ks = 0; ks < 5; ++ks)
#pragma unroll
        for (int cg = 0; cg < 4; ++cg) {
          const LAS char* q = bp + ((cg ^ key) * 32) + ks * 4096;
          const v2u lo = __builtin_bit_cast(v2u, __builtin_amdgcn_ds_read_tr16_b64_v4i16((LAS v4s*)q));
          const v2u hi = __builtin_bit_cast(v2u, __builtin_amdgcn_ds_read_tr16_b64_v4i16((LAS v4s*)(q + 2048)));
          bf[ks][cg] = v4u{lo.x, lo.y, hi.x, hi.y};
        }
      SB();
#pragma unroll
      for (int cg = 0; cg < 4; ++cg) acc[cg] = v4f{0.f, 0.f, 0.f, 0.f};
#pragma unroll
      for (int ks = 0; ks < 5; ++ks)
#pragma unroll
        for (int cg = 0; cg < 4; ++cg) acc[cg] = mfma(af[ks], bf[ks][cg], acc[cg]);
    }
    if (pw == 1) {
#pragma unroll
      for (int cg = 0; cg < 4; ++cg) {
        const v2u lo = __builtin_bit_cast(v2u, __builtin_amdgcn_ds_read_tr16_b64_v4i16((LAS v4s*)(bp + ((cg ^ key) * 32) + 5 * 4096)));
        acc[cg] = mfma(af[5], v4u{lo.x, lo.y, 0u, 0u}, acc[cg]);
      }
    }
    for (int i = 0; i < STAG * s; ++i) __builtin_amdgcn_s_sleep(1);
    st128(seal(v4f{acc[0][0], acc[1][0], acc[2][0], acc[3][0]}, tag), rwp, gout, 0);
    STAMP(3);
    const unsigned tpub = now();
    if (pw == 0) {
      pace<SETTLE>(tpub);
      lds_wait<2>(FL + FL_SLOT, NQ);
      v4f q;
#pragma nounroll
      for (int sp = 0; sp < SPIN_G; ++sp) {
        CBAR(); COUNT();
        v4u g[4];
#pragma unroll
        for (int i = 0; i < 4; ++i) g[i] = __builtin_amdgcn_raw_buffer_load_b128(rwq, lane * 16 + i * 1024, WS_Q + h * 4096, AUX_SC1);
        unsigned bad = 0;
        q = (unseal(g[0], tag, bad) + unseal(g[1], tag, bad)) + (unseal(g[2], tag, bad) + unseal(g[3], tag, bad));
        if (__all((bad & TAG_MASK) == 0u)) break;
      }
      STAMP(4);
      LAS unsigned short* qd = (LAS unsigned short*)(L + L_QS) + g4 * 64 + c;
      const unsigned q01 = cvtpk(q[0], q[1]), q23 = cvtpk(q[2], q[3]);
      qd[0] = (unsigned short)q01; qd[16] = (unsigned short)(q01 >> 16); qd[32] = (unsigned short)q23; qd[48] = (unsigned short)(q23 >> 16);
      LDS_DRAIN();
      FL[FL_Q] = 1u;
    } else {
      const unsigned short bias = ((LAS unsigned short*)(L + L_XR + 1024 + 768))[((j * 18) & 7) + min(lane, 17)];
      const int f = j * 18 + lane, lc = f >> 6;
      const int cnt = lane < 18 ? (576 * lc + 575) / 162 - (32 * lc) / 9 + 1 : 0;
      const int ha = WS_H + lc * 5 * 256 + (f & 15) * 16;
      pace<SETTLE>(tpub);
      lds_wait<2>(FL + FL_SLOT, NH);
      v4f hv;
#pragma nounroll
      for (int sp = 0; sp < SPIN_G; ++sp) {
        CBAR(); COUNT();
        v4u g[5];
#pragma unroll
        for (int k = 0; k < 5; ++k) g[k] = __builtin_amdgcn_raw_buffer_load_b128(rwh, k < cnt ? ha + k * 256 : OOB, 0, AUX_SC1);
        unsigned bad = 0;
        hv = v4f{0.f, 0.f, 0.f, 0.f};
#pragma unroll
        for (int k = 0; k < 5; ++k) hv += unseal(g[k], k < cnt ? tag : 0u, bad);
        if (__all((bad & TAG_MASK) == 0u)) break;
      }
      STAMP(4);
      const float hsum = (f & 32) ? ((f & 16) ? hv[3] : hv[2]) : ((f & 16) ? hv[1] : hv[0]);
      const float t = rbf(rbf(hsum) + asf((unsigned)bias << 16));
      const float z2 = 2.0f * 0.7978845608028654f * (t + 0.044715f * t * t * t);
      const float ge = t / (1.0f + __builtin_amdgcn_exp2f(-z2 * LOG2E));
      if (lane < 32) L[L_HS + lane] = lane < 18 ? cvtpk(ge, ge) : 0u;
      LDS_DRAIN();
      FL[FL_H] = 1u;
    }
    STAMP(5);
    VM_CLEAR();
  } else if (wave == W_C) {
    const unsigned tag32 = __builtin_amdgcn_raw_buffer_load_b32(rws, 0, 0, AUX_SC1) + 1u, tag = tag32 & TAG_MASK;
    lds_wait<1>(FL + FL_T0 + 1, 1u);
    unsigned t0 = __builtin_amdgcn_readfirstlane(FL[FL_T0]);
    const unsigned town = t0;
    auto mean_offset = [&](int off) {
      const v2u tt = __builtin_amdgcn_raw_buffer_load_b64(rws, WS_TT + off + c * 256, 0, AUX_SC1);
      const bool ok = tt.y == tag32;
      const float dsum = row_sum(ok ? (float)(int)(tt.x - town) : 0.f), dcnt = row_sum(ok ? 1.f : 0.f);
      float dv = ok ? (float)(int)(tt.x - town) : -1000.f;
      dv = fminf(dv, dpp<XOR1>(dv));
      const float dm = fmaxf(dsum * __builtin_amdgcn_rcpf(fmaxf(dcnt, 1.f)), row_max(dv) - (float)CAP);
      return __builtin_amdgcn_readfirstlane((int)fminf(fmaxf(dm, -150.f), 150.f));
    };
    if (BEACON) {
      if (lane == 0) poke(ws, WS_TB + (b >> 3) * 256 + (b & 7) * 8, town, tag32);
      v2u tb = {0u, 0u};
      if (lane < 8) tb = peek(ws, WS_TB + (b >> 3) * 256 + lane * 8);
      const float dmin = -row_max(tb.y == tag32 && lane < 8 ? (float)(int)(town - tb.x) : 0.f);
      t0 = town + min(max(__builtin_amdgcn_readfirstlane((int)dmin) + BIAS, -150), 0);
      *(volatile LAS v2u*)(FL + FL_T1) = v2u{t0, 1u};
    }
    if (s == 0) {
      lds_wait<1>(FL + FL_TS + 1, 1u);
      if (lane < 8) __builtin_amdgcn_raw_buffer_store_b64(v2u{FL[FL_TS], tag32}, rws, WS_TT + 128 + lane * 8, h * 256, AUX_SC1);
    }
    pace<TS>(town + (int)(t0 - town) / 2);
    t0 = town + mean_offset(128) - LEAD;
    *(volatile LAS v2u*)(FL + FL_T1) = v2u{t0, 2u};
    STAMP(1);
    lds_wait<4>(FL + FL_FW, 1u);
    const int hh = lane >> 5;
    v4f fw = *(LAS v4f*)(L + L_FW + hh * 4);
    int oaddr = WS_O + j * O_STRIDE + (hh * 128 + c * 8 + (g4 & 1) * 4) * 4;
    const v2u mlp = *(LAS v2u*)(L + L_G + (lane & 1) * 2);
    v4u mls = seal(v4f{asf(mlp.x), asf(mlp.y), asf(mlp.x), asf(mlp.y)}, tag);
    int maddr = lane < 4 ? WS_O + j * O_STRIDE + 1024 + lane * 16 : OOB;
    PIN(fw); PIN(oaddr); PIN(mls); PIN(maddr);
    pace<slot_time(44)>(t0);
    STAMP(2);
    v4f o;
#pragma nounroll
    for (int sp = 0; sp < SPIN_L; ++sp) {
      CBAR();
      v4u d[4];
#pragma unroll
      for (int w = 0; w < 4; ++w) d[w] = *(LAS v4u*)(L + L_ORED + w * 256 + lane * 4);
      o = __builtin_bit_cast(v4f, d[0]) * fw[0];
#pragma unroll
      for (int w = 1; w < 4; ++w) o += __builtin_bit_cast(v4f, d[w]) * fw[w];
      const float chk = (o[0] + o[1]) + (o[2] + o[3]);
      if (__all(chk == chk)) break;
      if (__all(full(d[0]) & full(d[1]) & full(d[2]) & full(d[3]))) break;
    }
    STAMP(3);
    st128(seal(o, tag), rws, oaddr, 0);
    st128(mls, rws, maddr, 0);
    STAMP(4);
    pace<TO>(now());
    FL[FL_OPUB] = 1u;
    VM_CLEAR();
  } else {
    const int aw = wave - W_A;
    *(LAS v4u*)(L + L_ORED + aw * 256 + lane * 4) = v4u{EMPTY, EMPTY, EMPTY, EMPTY};
    if (aw != 0) {
      __builtin_amdgcn_s_sleep(TOUCH);
      if (aw == 1) { touch2(vc, kvo, kvo); touch2(out, 0, 0); }
      else touch2(aw == 2 ? wo : w2, j * (aw == 2 ? 18432 : 20736), j * (aw == 2 ? 18432 : 20736) + 18400);
    }
    const bool reducer = aw == 1 && (j & 31) < 9;
    const int hb = c >> 3;
    lds_wait<4>(FL + FL_PROG + aw, 2u);
    lds_wait<1>(FL + FL_PROG + aw, 3u);
    STAMP(1);
    v4u kf[16];
#pragma unroll
    for (int t = 0; t < 4; ++t)
#pragma unroll
      for (int kk = 0; kk < 4; ++kk)
        kf[t * 4 + kk] = *(LAS v4u*)(L + L_K + (aw * 16 + t * 4 + (c & 3)) * 256 + (c >> 2) * 64 + (((kk * 4 + g4) ^ c) * 4));
#pragma unroll
    for (int i = 0; i < 16; ++i) PIN(kf[i]);
    lds_wait<0>(FL + FL_Q, 1u);
    STAMP(2);
    v4u qf[4];
#pragma unroll
    for (int kk = 0; kk < 4; ++kk) qf[kk] = *(LAS v4u*)(L + L_QS + hb * 64 + (kk * 4 + g4) * 4);
    SB();
    v4f sc[4];
#pragma unroll
    for (int t = 0; t < 4; ++t) sc[t] = v4f{0.f, 0.f, 0.f, 0.f};
#pragma unroll
    for (int t = 0; t < 4; ++t)
#pragma unroll
      for (int kk = 0; kk < 4; ++kk) sc[t] = mfma(kf[t * 4 + kk], qf[kk], sc[t]);
    float sv[4];
#pragma unroll
    for (int e = 0; e < 4; ++e) {
      const float lo = (c & 1) ? sc[1][e] : sc[0][e], hi = (c & 1) ? sc[3][e] : sc[2][e];
      sv[e] = rbf(rbf((c & 2) ? hi : lo) * 0.08838834764831845f);
    }
    float m = fmaxf(fmaxf(sv[0], sv[1]), fmaxf(sv[2], sv[3]));
    m = fmaxf(m, dpp<XOR1>(m)); m = fmaxf(m, dpp<XOR2>(m));
    m = xrow_max(m);
    float p[4];
    v4u pk;
#pragma unroll
    for (int e = 0; e < 4; ++e) { p[e] = __builtin_amdgcn_exp2f((sv[e] - m) * LOG2E); pk[e] = cvtpk(p[e], p[e]); }
    *(LAS v4u*)(L + L_P + aw * 128 + (hb * 4 + g4) * 16 + (c & 3) * 4) = pk;
    float l = (p[0] + p[1]) + (p[2] + p[3]);
    l += dpp<XOR1>(l); l += dpp<XOR2>(l);
    l = xrow_sum(l);
    if ((lane & 55) == 0) *(LAS v2u*)(L + L_ML + aw * 4 + hb * 2) = v2u{asu(m), asu(l)};
    LDS_DRAIN();
    FL[FL_P + aw] = 1u;
    STAMP(3);
    const unsigned tag32 = L[L_XR + 512] + 1u, tag = tag32 & TAG_MASK;
    if (aw == 0) {
      lds_wait4<0>(FL + FL_P, 1u);
      const int hh = (lane >> 2) & 1, w = lane & 3;
      const v2u ml = *(LAS v2u*)(L + L_ML + w * 4 + hh * 2);
      float M = asf(ml.x);
      M = fmaxf(M, dpp<XOR1>(M)); M = fmaxf(M, dpp<XOR2>(M));
      const float fw = __builtin_amdgcn_exp2f((asf(ml.x) - M) * LOG2E);
      float Ls = asf(ml.y) * fw;
      Ls += dpp<XOR1>(Ls); Ls += dpp<XOR2>(Ls);
      if (lane < 8) L[L_FW + lane] = asu(fw);
      if ((lane & 59) == 0) *(LAS v2u*)(L + L_G + hh * 2) = v2u{asu(M), asu(Ls)};
      LDS_DRAIN();
      FL[FL_FW] = 1u;
      STAMP(4);
    } else if (reducer) {
      const int red = (j >> 5) * 9 + (j & 31);
      const int ocol = red * 8 + (lane & 1) * 4 + g4;
      const unsigned xb = ((LAS unsigned short*)(L + L_XR))[ocol];
      const unsigned bb = ((LAS unsigned short*)(L + L_XR + 768))[ocol & 7];
      float base = asf(xb << 16) + asf(bb << 16);
      PIN(base);
      lds_wait<4>(FL + FL_OPUB, 1u);
      lds_wait<0>(FL + FL_FPUB, 5u);
      pace<TF>(now());
      STAMP(4);
      v4f sum;
      bool done = false;
#pragma nounroll
      for (int sp = 0; sp < SPIN_G; ++sp) {
        CBAR(); COUNT();
        if (!done) {
          v4u pv[8];
#pragma unroll
          for (int i = 0; i < 8; ++i) pv[i] = __builtin_amdgcn_raw_buffer_load_b128(rws, (lane >> 1) * 64 + (lane & 1) * 16 + (i & 1) * 2048, WS_F + (red >> 1) * F_STRIDE + (red & 1) * 32 + (i >> 1) * 4096, AUX_SC1);
          unsigned bad = 0;
          sum = unseal(pv[0], tag, bad);
#pragma unroll
          for (int i = 1; i < 8; ++i) sum += unseal(pv[i], tag, bad);
          done = (bad & TAG_MASK) == 0u;
        }
        if (__all(done)) break;
      }
      STAMP(5);
      auto s02 = __builtin_amdgcn_permlane32_swap(asu(sum[0]), asu(sum[2]), false, false);
      auto s13 = __builtin_amdgcn_permlane32_swap(asu(sum[1]), asu(sum[3]), false, false);
      const float u0 = asf(s02[0]) + asf(s02[1]), u1 = asf(s13[0]) + asf(s13[1]);
      auto s01 = __builtin_amdgcn_permlane16_swap(asu(u0), asu(u1), false, false);
      float u = asf(s01[0]) + asf(s01[1]);
      u += dpp<ROR8>(u); u += dpp<ROR4>(u); u += dpp<XOR2>(u);
      u += base;
      if ((lane & 14) == 0) __builtin_amdgcn_raw_buffer_store_b16((unsigned short)cvtpk(u, u), RS(out, D * 2), ocol * 2, 0, 0);
      if (j == 0 && lane == 0) __builtin_amdgcn_raw_buffer_store_b32(tag32, rws, 0, 0, AUX_SC1);
      STAMP(6);
    }
    VM_CLEAR();
  }

}

extern "C" __device__ __attribute__((used, noinline)) void fused_decode_end() {}

extern "C" void kernel_launch(void* const* d_in, const int* in_sizes, int n_in, void* d_out, int out_size, void* d_ws, size_t ws_size,
                              hipStream_t stream) {
  fused_decode<<<256, NWAVE * 64, 0, stream>>>(d_in[3], d_in[1], d_in[6], d_in[2], d_in[4], d_in[8], d_ws, d_in[0], d_in[7], d_in[9], d_out);
}
```

```cpp
#include <hip/hip_runtime.h>
#include <stdint.h>
#include <type_traits>

typedef unsigned v4u __attribute__((ext_vector_type(4)));
typedef unsigned v2u __attribute__((ext_vector_type(2)));
typedef float v4f __attribute__((ext_vector_type(4)));
typedef float v2f __attribute__((ext_vector_type(2)));
typedef short v4s __attribute__((ext_vector_type(4)));
typedef __bf16 bf16x8 __attribute__((ext_vector_type(8)));
typedef __bf16 bf16x2 __attribute__((ext_vector_type(2)));
#define LAS __attribute__((address_space(3)))

constexpr int D = 576, SK = 16384, WIN = 4096, F = 4608;
constexpr int KV_BYTES = 16 * SK * 128 * 2;
constexpr int WQ_BYTES = D * 4096 * 2, W1_BYTES = D * F * 2;
constexpr int WS_TT = 4096  , WS_TB = 8192  , WS_Q = 16384, WS_H = WS_Q + 256 * 256, WS_O = WS_H + 72 * 5 * 256, O_STRIDE = 1152,
              WS_F = WS_O + 256 * O_STRIDE, F_STRIDE = 256 * 64 + 256, WS_END = WS_F + 36 * F_STRIDE;
constexpr unsigned EMPTY = 0xFFFFFFFFu;
constexpr int OOB = 0x7FFFFFFF;
#define WINDOW 11
#define PACE10 143
#define PACE0 80
#define BLIND 3
#define AHEAD 6
#define LAG 3
#define LAG1 4
#define LAG2 7
#define LAG3 5
#define TQ (slot_time(27))
#define TH (slot_time(31))
#define TM (slot_time(48))
#define EARLY AHEAD
#define DRIFT0 (SMP + 19)
#define TS (slot_time(SMP + 13))
#define SYNC 1
#define OATOM 0
#define TOUCH 60
#define EXTRA 0
#define STAG 0
#define PV0 42
#define TG 15
#define F4S 43
#define F4D 1
#define FCOST 92
#define SMP 12
#define RTT0 55
#define HAT 47
#define WE 11
#define WEN 28
#define NQ 27
#define NH 33
#define TA (slot_time(9))
#define GAP 200
#define KVORD 1
#define CAP 35
#define OG 49
#define BEACON 0
#define BIAS 35
#define LEAD 0
#define DRIFTA 12
#define OUT_AUX 0
#define EP_AUX 0
#define SETTLE 60
#define TO 35
#define TF 25
constexpr int AUX_NT = 2, AUX_SC1 = 16, W = WINDOW;
#define ABL 0
constexpr int SPIN_G = ABL ? 1 : 20000, SPIN_L = 400000;
constexpr float LOG2E = 1.4426950408889634f;

constexpr int L_WQ = 0  , L_K = 5120  , L_W1 = L_K + 16384  , L_FLAG = L_W1 + 6144  ;
constexpr int FL_Q = 0, FL_H = 1, FL_PROG = 4  , FL_P = 8  , FL_KDONE = 13, FL_FW = 14, FL_G = 15, FL_VDONE = 16, FL_OPUB = 17,
              FL_FPUB = 18, FL_T0 = 20  , FL_T1 = 22  , FL_TS = 24  , FL_SLOT = 26;
constexpr int L_XR = L_FLAG + 32  , L_Y = L_XR + 2 * 1024  , L_QS = L_Y + 2 * 512  , L_HS = L_QS + 128  ,
              L_ML = L_HS + 32  , L_FW = L_ML + 16  , L_G = L_FW + 16  , L_OS = L_G + 16  , L_ORED = L_OS + 16  ,
              L_P = L_ORED + 1280  , L_END = L_P + 512;
constexpr int YZ = 192;

#define RS(p, n) __builtin_amdgcn_make_buffer_rsrc((void*)(p), 0, (int)(n), 0x00020000)
#define SB() __builtin_amdgcn_sched_barrier(0)
#define CBAR() asm volatile("" ::: "memory")
#define LDS_DRAIN() asm volatile("s_waitcnt lgkmcnt(0)" ::: "memory")
#define VM_DRAIN() asm volatile("s_waitcnt vmcnt(0)" ::: "memory")
#define VM_CLEAR() __builtin_amdgcn_s_waitcnt(0x0F70)
#define PIN(x) asm volatile("" : "+v"(x))

#define COUNT() do {} while (0)
#define STAMP(i) do {} while (0)

__device__ __forceinline__ float asf(unsigned u) { return __builtin_bit_cast(float, u); }
__device__ __forceinline__ unsigned asu(float f) { return __builtin_bit_cast(unsigned, f); }
__device__ __forceinline__ unsigned cvtpk(float lo, float hi) {
  return __builtin_bit_cast(unsigned, __builtin_convertvector(v2f{lo, hi}, bf16x2));
}
__device__ __forceinline__ v2f unpk(unsigned p) { return v2f{asf(p << 16), asf(p & 0xFFFF0000u)}; }
__device__ __forceinline__ float rbf(float f) { return asf(cvtpk(0.f, f)); }
__device__ __forceinline__ float vmax(float a, float b) { float r; asm("v_max_f32 %0, %1, %2" : "=v"(r) : "v"(a), "v"(b)); return r; }
__device__ __forceinline__ float vmax3(float a, float b, float c) { float r; asm("v_max3_f32 %0, %1, %2, %3" : "=v"(r) : "v"(a), "v"(b), "v"(c)); return r; }
template <int CTRL> __device__ __forceinline__ float dpp(float x) {
  return asf(__builtin_amdgcn_mov_dpp(asu(x), CTRL, 0xf, 0xf, true));
}
constexpr int XOR1 = 0xB1, XOR2 = 0x4E, XOR7 = 0x141, ROR8 = 0x128, ROR4 = 0x124;
__device__ __forceinline__ float xrow_sum(float x) {
  auto s = __builtin_amdgcn_permlane16_swap(asu(x), asu(x), false, false);
  x = asf(s[0]) + asf(s[1]);
  auto t = __builtin_amdgcn_permlane32_swap(asu(x), asu(x), false, false);
  return asf(t[0]) + asf(t[1]);
}
__device__ __forceinline__ float xrow_max(float x) {
  auto s = __builtin_amdgcn_permlane16_swap(asu(x), asu(x), false, false);
  x = fmaxf(asf(s[0]), asf(s[1]));
  auto t = __builtin_amdgcn_permlane32_swap(asu(x), asu(x), false, false);
  return fmaxf(asf(t[0]), asf(t[1]));
}
__device__ __forceinline__ float row_sum(float x) {
  x += dpp<XOR1>(x); x += dpp<XOR2>(x); x += dpp<XOR7>(x); x += dpp<ROR8>(x);
  return x;
}
__device__ __forceinline__ float row_max(float x) {
  asm("s_nop 1\n\tv_max_f32_dpp %0, %0, %0 quad_perm:[1,0,3,2] row_mask:0xf bank_mask:0xf\n\ts_nop 1\n\t"
      "v_max_f32_dpp %0, %0, %0 quad_perm:[2,3,0,1] row_mask:0xf bank_mask:0xf\n\ts_nop 1\n\t"
      "v_max_f32_dpp %0, %0, %0 row_half_mirror row_mask:0xf bank_mask:0xf\n\ts_nop 1\n\t"
      "v_max_f32_dpp %0, %0, %0 row_ror:8 row_mask:0xf bank_mask:0xf" : "+v"(x));
  return x;
}
__device__ __forceinline__ v4f mfma(v4u a, v4u b, v4f c) {
  return __builtin_amdgcn_mfma_f32_16x16x32_bf16(__builtin_bit_cast(bf16x8, a), __builtin_bit_cast(bf16x8, b), c, 0, 0, 0);
}
__device__ __forceinline__ v4u box(v4f v) {
  v4u r;
#pragma unroll
  for (int i = 0; i < 4; ++i) r[i] = min(asu(v[i]), 0xFFFFFFFEu);
  return r;
}
constexpr unsigned TAG_MASK = 0x3FFu;
__device__ __forceinline__ v4u seal(v4f v, unsigned tag) {
  v4u r;
#pragma unroll
  for (int i = 0; i < 4; ++i) r[i] = (asu(v[i]) & ~TAG_MASK) | tag;
  return r;
}
constexpr unsigned TAG8 = 0xFFu;
__device__ __forceinline__ v4u seal8(v4f v, unsigned tag) {
  v4u r;
#pragma unroll
  for (int i = 0; i < 4; ++i) { const unsigned w = asu(v[i]); r[i] = (((w & 0x7F800000u) == 0x7F800000u ? w : w + 0x80u) & ~TAG8) | tag; }
  return r;
}
__device__ __forceinline__ v4f unseal(v4u w, unsigned tag, unsigned& bad) {
  const v4u x = w ^ tag;
  bad |= (x.x | x.y) | (x.z | x.w);
  return __builtin_bit_cast(v4f, x);
}
__device__ __forceinline__ bool full(v4u v) { return max(max(v.x, v.y), max(v.z, v.w)) != EMPTY; }
template <int NAP = 2> __device__ __forceinline__ void lds_wait(volatile LAS unsigned* f, unsigned need) {
#pragma nounroll
  for (int sp = 0; sp < SPIN_L; ++sp) {
    if ((unsigned)__builtin_amdgcn_readfirstlane(*f) >= need) break;
    if (NAP) __builtin_amdgcn_s_sleep(NAP);
  }
  CBAR();
}
template <int NAP> __device__ __forceinline__ void lds_wait4(volatile LAS unsigned* f, unsigned need) {
#pragma nounroll
  for (int sp = 0; sp < SPIN_L; ++sp) {
    const v4u v = *(volatile LAS v4u*)f;
    if ((unsigned)__builtin_amdgcn_readfirstlane(min(min(v.x, v.y), min(v.z, v.w))) >= need) break;
    if (NAP) __builtin_amdgcn_s_sleep(NAP);
  }
  CBAR();
}
__device__ __forceinline__ void lds_inc(LAS unsigned* f, int lane) {
  if (lane == 0) __hip_atomic_fetch_add(f, 1u, __ATOMIC_RELAXED, __HIP_MEMORY_SCOPE_WORKGROUP);
}
typedef __attribute__((address_space(1))) unsigned long long gu64;
__device__ __forceinline__ v2u peek(void* ws, int off) {
  unsigned long long zero = 0ull;
  asm volatile("" : "+v"(zero));
  const unsigned long long r = __hip_atomic_fetch_or((gu64*)((char*)ws + off), zero, __ATOMIC_RELAXED, __HIP_MEMORY_SCOPE_AGENT);
  return v2u{(unsigned)r, (unsigned)(r >> 32)};
}
__device__ __forceinline__ void touch_and_barrier(const void* p, unsigned o0, unsigned o1, unsigned o2, const void* q, unsigned o3) {
  unsigned d0, d1, d2, d3;
  asm volatile("s_load_dword %0, %4, %5\n\ts_load_dword %1, %4, %6\n\ts_load_dword %2, %4, %7\n\ts_load_dword %3, %8, %9\n\ts_barrier\n\ts_waitcnt lgkmcnt(0)"
               : "=&s"(d0), "=&s"(d1), "=&s"(d2), "=&s"(d3) : "s"(p), "s"(o0), "s"(o1), "s"(o2), "s"(q), "s"(o3) : "memory");
}
__device__ __forceinline__ void touch2(const void* p, unsigned o0, unsigned o1) {
  unsigned d0, d1;
  asm volatile("s_load_dword %0, %2, %3\n\ts_load_dword %1, %2, %4\n\ts_waitcnt lgkmcnt(0)" : "=&s"(d0), "=&s"(d1) : "s"(p), "s"(o0), "s"(o1) : "memory");
}
__device__ __forceinline__ void poke(void* ws, int off, unsigned lo, unsigned hi) {
  (void)__hip_atomic_exchange((gu64*)((char*)ws + off), ((unsigned long long)hi << 32) | lo, __ATOMIC_RELAXED, __HIP_MEMORY_SCOPE_AGENT);
}
__device__ __forceinline__ unsigned now() {
  unsigned long long t;
  asm volatile("s_memrealtime %0\n\ts_waitcnt lgkmcnt(0)" : "=s"(t)::"memory");
  return (unsigned)t;
}
template <int T> __device__ __forceinline__ void pace(unsigned t0) {
  asm volatile("1:\n\ts_memrealtime s[90:91]\n\ts_waitcnt lgkmcnt(0)\n\ts_sub_u32 s90, s90, %0\n\ts_cmpk_lt_u32 s90, %1\n\ts_cbranch_scc1 1b"
               ::"s"(__builtin_amdgcn_readfirstlane(t0)), "i"(T) : "memory", "scc", "s90", "s91");
}
constexpr int slot_cost(int n) { return n == 10 ? 30 : n == 42 ? GAP : (n >= 43 ? (n == 47 ? 60 : FCOST) : 100); }
constexpr int slot_time(int n) {
  if (n < AHEAD) return (n - BLIND + 1) * 15;
  int c = 0;
  for (int i = AHEAD; i < n; ++i) c += slot_cost(i);
  return PACE0 + c * PACE10 / 1000;
}
constexpr int pv_done(int n) { return n < PV0 ? 0 : ((n - PV0) * 5 + 2 < n - 26 - LAG ? (n - PV0) * 5 + 2 : n - 26 - LAG); }
template <int N> __device__ __forceinline__ void vmwait() { asm volatile("s_waitcnt vmcnt(%0)" ::"i"(N) : "memory"); }
__device__ __forceinline__ void st128(v4u d, __amdgpu_buffer_rsrc_t r, int voff, int soff) {
  __builtin_amdgcn_raw_buffer_store_b128(d, r, voff, soff, AUX_SC1);
  asm volatile("s_nop 1" ::"v"(d) : "memory");
}
__device__ __forceinline__ void dma(__amdgpu_buffer_rsrc_t r, int voff, int soff, int ldsaddr) {
  asm volatile("s_mov_b32 m0, %0\n\ts_nop 0\n\tbuffer_load_dwordx4 %1, %2, %3 offen nt lds" ::"s"(ldsaddr), "v"(voff), "s"(r), "s"(soff) : "memory");
}
__device__ __forceinline__ void dma_dev(__amdgpu_buffer_rsrc_t r, int voff, int ldsaddr) {
  asm volatile("s_mov_b32 m0, %0\n\ts_nop 0\n\tbuffer_load_dwordx4 %1, %2, 0 offen sc1 lds" ::"s"(ldsaddr), "v"(voff), "s"(r) : "memory");
}
__device__ __forceinline__ v4u keep_mask(int m) {
  v4u k;
#pragma unroll
  for (int v = 0; v < 4; ++v) k[v] = ((m >> 1) == v) ? ((m & 1) ? 0xFFFF0000u : 0x0000FFFFu) : 0u;
  return k;
}
template <int I, int N, class Fn> __device__ __forceinline__ void static_for(Fn&& fn) {
  if constexpr (I < N) { fn(std::integral_constant<int, I>{}); static_for<I + 1, N>(fn); }
}
__device__ __forceinline__ void layernorm(unsigned (&p)[5], int lane) {
  const float c0 = asf(__builtin_amdgcn_readfirstlane(p[0]) << 16);
  v2f v[5], d[5];
#pragma unroll
  for (int i = 0; i < 5; ++i) { v[i] = unpk(p[i]); d[i] = v[i] - c0; }
  if (lane >= 32) d[4] = v2f{0.f, 0.f};
  const v2f s1v = (d[0] + d[1]) + (d[2] + d[3]) + d[4];
  v2f s2v = d[0] * d[0];
#pragma unroll
  for (int i = 1; i < 5; ++i) s2v = __builtin_elementwise_fma(d[i], d[i], s2v);
  const float s1 = xrow_sum(row_sum(s1v.x + s1v.y)) * (1.0f / D), s2 = xrow_sum(row_sum(s2v.x + s2v.y)) * (1.0f / D);
  const float var = fmaxf(fmaf(-s1, s1, s2), 0.f);
  const float mb = rbf(c0 + s1), sd = rbf(__builtin_amdgcn_sqrtf(rbf(rbf(var) + 1e-5f)));
  const float inv = __builtin_amdgcn_rcpf(sd);
#pragma unroll
  for (int i = 0; i < 5; ++i) {
    const v2f t = v[i] - mb;
    const v2f u = v2f{rbf(t.x), rbf(t.y)} * inv;
    p[i] = cvtpk(u.x, u.y);
  }
}

constexpr int W_F4 = 4, W_C = 5, W_P = 6, W_A = 8, NWAVE = 12;

extern "C" __global__ void __launch_bounds__(NWAVE * 64, 3)
fused_decode(const void* __restrict__ wq, const void* __restrict__ kc, const void* __restrict__ w1, const void* __restrict__ vc,
             const void* __restrict__ wo, const void* __restrict__ w2, void* __restrict__ ws, const void* __restrict__ x,
             const void* __restrict__ b1, const void* __restrict__ b2, void* __restrict__ out) {
  __shared__ __attribute__((aligned(16))) unsigned smem[L_END];
  LAS unsigned* L = (LAS unsigned*)smem;
  volatile LAS unsigned* FL = (volatile LAS unsigned*)smem + L_FLAG;
  const int tid = threadIdx.x, lane = tid & 63, wave = __builtin_amdgcn_readfirstlane(tid >> 6);
  const int b = blockIdx.x, j = (b & 7) * 32 + (b >> 3), h = j >> 4, s = j & 15;
  const int r8 = lane >> 3, c8 = lane & 7, g4 = lane >> 4, c = lane & 15;
  const int lcA = (j * 9) >> 5, rowA = 18 * ((j * 9) & 31);
  const int lds0 = (int)(unsigned)(uintptr_t)L;
  if (!(ABL & 64) && (wave >> 1) == (W_P >> 1)) {
    const int dst = lds0 + (L_XR + (wave & 1) * 1024) * 4;
    dma_dev(RS(x, D * 2), lane * 16, dst);
    dma_dev(RS(x, D * 2), lane * 16 + 1024, dst + 1024);
    dma_dev(RS(ws, 16), lane * 16, dst + 2048);
    const int red8 = ((j >> 5) * 9 + min(j & 31, 8)) * 16;
    dma_dev(RS((wave & 1) ? (const char*)b1 : (const char*)b2, (wave & 1) ? F * 2 : D * 2), lane < 4 ? lane * 16 + ((wave & 1) ? (j * 36) & ~15 : red8) : OOB, dst + 3072);
  }
  if (!(ABL & 128) && wave == W_C) {
    unsigned long long pc;
    unsigned pcoff, len;
    asm volatile("s_getpc_b64 %0\n.Lpc%=:\n\ts_mov_b32 %1, .Lpc%=-fused_decode\n\ts_mov_b32 %2, fused_decode_end-fused_decode"
                 : "=s"(pc), "=s"(pcoff), "=s"(len));
    dma_dev(RS(pc - pcoff, len), lane < 40 ? lane * 16 + (b >> 3) * 640 : OOB, lds0 + (L_ORED + 1024) * 4);
  }
  const unsigned kvo = (h * SK + (SK - WIN) + s * 16) * 256;
  if (wave == W_A) {
    const unsigned c1 = lcA * 128;
    touch_and_barrier(w1, rowA * 9216 + c1, min(rowA + 161, D - 1) * 9216 + c1, c1, kc, kvo);
  } else if (wave >= 4) {
    if (wave == W_F4 && lane < 32) L[L_FLAG + lane] = 0u;
    LDS_DRAIN();
    __builtin_amdgcn_s_barrier();
    CBAR();
  }
  const auto rws = RS(ws, WS_END);
  const auto rwq = RS(ws, (ABL & 4) ? 0 : WS_END), rwh = RS(ws, (ABL & 8) ? 0 : WS_END), rwm = RS(ws, (ABL & 16) ? 0 : WS_END), rwp = RS(ws, (ABL & 32) ? 0 : WS_END);

  if (__builtin_expect(wave <= W_F4, 1)) {
    v4u wf[9], ha[5], mskf, mw, ow;
    int vo, v2, faddr, oga, mga;
    unsigned tag;
    auto prep_addr = [&]() {
      const bool live = wave < 4 || c < 8;
      vo = live ? g4 * 4608 + wave * 256 + c * 16 : OOB - 4096;
      v2 = live ? g4 * 1152 + wave * 256 + c * 16 : OOB - 5 * 4608;
      faddr = lane < 32 && live ? WS_F + (wave * 8 + (c >> 1)) * F_STRIDE + j * 64 + (c & 1) * 32 + g4 * 16 : OOB;
      oga = WS_O + (h * 16 + c) * O_STRIDE + s * 64 + g4 * 16;
      mga = WS_O + 1024 + (h * 16 + c) * O_STRIDE + (s >> 3) * 16;
      mskf = keep_mask(c);
      PIN(mga); PIN(vo); PIN(v2); PIN(faddr); PIN(oga); PIN(mskf);
    };
    auto prep_h = [&]() {
      lds_wait<0>(FL + FL_H, 1u);
      unsigned hv[5];
      const unsigned ep = L[L_XR + 512];
#pragma unroll
      for (int rg = 0; rg < 5; ++rg) hv[rg] = L[L_HS + rg * 4 + g4];
      SB();
      tag = (ep + 1u) & TAG_MASK;
#pragma unroll
      for (int rg = 0; rg < 5; ++rg) ha[rg] = mskf & hv[rg];
      asm volatile("" : "+v"(tag), "+v"(ha[0]), "+v"(ha[1]), "+v"(ha[2]), "+v"(ha[3]), "+v"(ha[4]));
    };
    if (__builtin_expect(wave < 4, 1)) {
      const auto rq = RS(wq, WQ_BYTES), rk = RS(kc, KV_BYTES), rv = RS(vc, KV_BYTES), r1 = RS(w1, W1_BYTES);
      const int qs = (s >> 2) * 144 * 8192 + h * 512 + (s & 3) * 128;
      const int kvb = (h * SK + (SK - WIN) + s * 16) * 256 + wave * 1024, rot = h;
      const int cs = (c8 ^ (r8 & 6)) * 16;
      __amdgpu_buffer_rsrc_t ro, r2;
      v4u vv[16], pk[4], mskv, alast;
      v4f acc = {0.f, 0.f, 0.f, 0.f};
      unsigned t0;
      int dacc = 0, dstep = 0;
      static_for<0, 52>([&](auto nc) {
        constexpr int n = nc;
        if (n == 1) t0 = now();
        if (n == BLIND) __builtin_amdgcn_s_barrier();
        if (n == AHEAD) {
          vmwait<AHEAD - 1>();
          const unsigned t1 = now() - slot_time(n);
          if ((int)(t1 - t0) > 0) t0 = t1 + min((int)(t1 - t0), EXTRA);
          if (wave == 0) *(volatile LAS v2u*)(FL + FL_T0) = v2u{t0, 1u};
        }
        if (n < WEN) { if (n >= WE) vmwait<WE - 1>(); } else vmwait<W - 1>();
        if constexpr (n >= DRIFTA && n < 43) dacc += dstep;
        if constexpr (n >= DRIFTA) pace<slot_time(n)>(t0 + ((dacc + 128) >> 8));
        else if constexpr (n >= BLIND) pace<slot_time(n)>(t0);
        if constexpr (n < 5) {
          dma(rq, min((n * 4 + wave) * 8 + r8, 143) * 8192 + cs, qs, lds0 + L_WQ * 4 + (n * 4 + wave) * 1024);
        } else if constexpr (n < 11) {
          constexpr int i = n - 5;
          const unsigned v = (unsigned)((rowA + min((i * 4 + wave) * 8 + r8, 161)) * 9216 + cs);
          dma(r1, (int)min(v, v - (unsigned)(W1_BYTES - 128)), lcA * 128, lds0 + L_W1 * 4 + (i * 4 + wave) * 1024);
        } else if constexpr (n < 27) {
          constexpr int i = n - 11;
          dma(rk, g4 * 256 + ((c ^ (g4 * 4 + (i & 3))) * 16), kvb + (((KVORD ? (i & 3) * 4 + (i >> 2) : i) + rot) & 15) * 65536, lds0 + L_K * 4 + (wave * 16 + i) * 1024);
        } else if constexpr (n < 43) {
          constexpr int i = n - 27;
          vv[i] = __builtin_amdgcn_raw_buffer_load_b128(rv, lane * 16, kvb + (((KVORD ? (i & 3) * 4 + (i >> 2) : i) + rot) & 15) * 65536, AUX_NT);
        } else if constexpr (n < 48) {
          wf[n - 43] = __builtin_amdgcn_raw_buffer_load_b128(r2, v2 + (n - 43) * 4608, 0, AUX_NT);
        } else {
          wf[n - 43] = __builtin_amdgcn_raw_buffer_load_b128(ro, vo, (n - 48) * 1152, AUX_NT);
        }
        SB();
        if (n == AHEAD) STAMP(7);
        if (n == 4 + LAG1) { vmwait<LAG1>(); FL[FL_PROG + wave] = 1u; STAMP(1); }
        if (n == 10 + LAG2) { vmwait<LAG2>(); FL[FL_PROG + wave] = 2u; }
        if (n == 26 + LAG3) { vmwait<LAG3>(); FL[FL_PROG + wave] = 3u; STAMP(2); }
        if (n == SMP + 3 && wave == 0) {
          if (s == 0) {
            vmwait<3>();
            const unsigned t1 = now() - (slot_time(SMP) + RTT0);
            *(volatile LAS v2u*)(FL + FL_TS) = v2u{(int)(t1 - t0) > 0 ? t1 : t0, 1u};
          }
        }
        if (n == 21) prep_addr();
        if (n == 22) {
          mskv = keep_mask(c8);
          PIN(mskv);
          ro = RS((const char*)wo + j * 18432, 18432);
          r2 = RS((const char*)w2 + j * 20736, 20736);
          asm volatile("" : "+s"(ro), "+s"(r2));
        }
        if ((n == NQ || n == NH) && wave == 0) FL[FL_SLOT] = n;
        if (n == DRIFTA - 1 || n == DRIFTA + 2 || n == DRIFT0 - 1 || n == DRIFT0 + 2 || n == 37) {
          const v2u t1 = *(volatile LAS v2u*)(FL + FL_T1);
          if (__builtin_amdgcn_readfirstlane(t1.y)) dstep = (((int)(__builtin_amdgcn_readfirstlane(t1.x) - t0) * 256 - dacc) * (4096 / (n < 42 ? 42 - n : 1))) >> 12;
        }
        if (n == PV0) {
          lds_wait<0>(FL + FL_P + wave, 1u);
          STAMP(3);
#pragma unroll
          for (int t = 0; t < 4; ++t) pk[t] = *(LAS v4u*)(L + L_P + wave * 128 + ((c >> 3) * 4 + g4) * 16 + t * 4);
          alast = mskv & pk[3][3];
          PIN(alast);
        }
        if constexpr (n >= PV0 && n <= 42 + LAG) {
          constexpr int done = pv_done(n - 1), ready = pv_done(n);
#pragma unroll
          for (int i = done; i < ready; ++i) acc = mfma(i == 15 ? alast : mskv & pk[i >> 2][i & 3], vv[i], acc);
          if (n == 42 + LAG) {
            *(LAS v4u*)(L + L_ORED + wave * 256 + lane * 4) = box(acc);
            STAMP(4);
          }
        }
        if (n == HAT) prep_h();
        if (n == OG) {
          lds_wait<0>(FL + FL_OPUB, 1u);
          STAMP(5);
          mw = __builtin_amdgcn_raw_buffer_load_b128(rws, mga, 0, AUX_SC1);
          ow = __builtin_amdgcn_raw_buffer_load_b128(rws, oga, 0, AUX_SC1);
        }
        SB();
      });
    } else {
      prep_addr();
      const auto ro = RS((const char*)wo + j * 18432, 18432), r2 = RS((const char*)w2 + j * 20736, 20736);
      lds_wait<4>(FL + FL_T1 + 1, 2u);
      const unsigned t0 = __builtin_amdgcn_readfirstlane(FL[FL_T1]);
      static_for<0, 9>([&](auto nc) {
        constexpr int n = nc;
        pace<slot_time(F4S + n * F4D)>(t0);
        if constexpr (n < 5) asm volatile("buffer_load_dwordx4 %0, %1, %2, 0 offen nt" : "=v"(wf[n]) : "v"(v2 + n * 4608), "s"(r2) : "memory");
        else asm volatile("buffer_load_dwordx4 %0, %1, %2, 0 offen offset:%3 nt" : "=v"(wf[n]) : "v"(vo), "s"(ro), "i"((n - 5) * 1152) : "memory");
        SB();
        if (n + 43 == HAT) prep_h();
        if (n + 43 == OG) {
          lds_wait<0>(FL + FL_OPUB, 1u);
          STAMP(5);
          asm volatile("buffer_load_dwordx4 %0, %2, %4, 0 offen sc1\n\tbuffer_load_dwordx4 %1, %3, %4, 0 offen sc1"
                       : "=&v"(mw), "=&v"(ow) : "v"(mga), "v"(oga), "s"(rws) : "memory");
        }
        SB();
      });
    }
    v4f a2 = {0.f, 0.f, 0.f, 0.f};
#pragma unroll
    for (int rg = 0; rg < 5; ++rg) a2 = mfma(ha[rg], wf[rg], a2);
    PIN(a2);
    SB();
    unsigned bad = 0;
    v4f ml = unseal(mw, tag, bad), u = unseal(ow, tag, bad);
    COUNT();
    if (__builtin_expect(!__all((bad & TAG_MASK) == 0u), 0)) {
#pragma nounroll
      for (int sp = 0; sp < SPIN_G; ++sp) {
        COUNT();
        CBAR();
        bad = 0;
        ml = unseal(__builtin_amdgcn_raw_buffer_load_b128(rws, mga, 0, AUX_SC1), tag, bad);
        u = unseal(__builtin_amdgcn_raw_buffer_load_b128(rws, oga, 0, AUX_SC1), tag, bad);
        if (__all((bad & TAG_MASK) == 0u)) break;
      }
    }
    STAMP(6);
    const float gw = __builtin_amdgcn_exp2f((ml[0] - row_max(ml[0])) * LOG2E);
    float r[5] = {u[0] * gw, u[1] * gw, u[2] * gw, u[3] * gw, ml[1] * gw};
#pragma unroll
    for (int i = 4; i >= 0; --i) r[i] += dpp<XOR1>(r[i]);
#pragma unroll
    for (int i = 4; i >= 0; --i) r[i] += dpp<XOR2>(r[i]);
#pragma unroll
    for (int i = 4; i >= 0; --i) r[i] += dpp<XOR7>(r[i]);
#pragma unroll
    for (int i = 4; i >= 0; --i) r[i] += dpp<ROR8>(r[i]);
    const float inv = __builtin_amdgcn_rcpf(r[4]);
    v4u oa[4];
#pragma unroll
    for (int rg = 0; rg < 4; ++rg) oa[rg] = mskf & cvtpk(r[rg] * inv, r[rg] * inv);
    SB();
#pragma unroll
    for (int rg = 0; rg < 4; ++rg) a2 = mfma(oa[rg], wf[5 + rg], a2);
    st128(seal(a2, tag), rws, faddr, 0);
    lds_inc(L + L_FLAG + FL_FPUB, lane);
    if (wave != 3) STAMP(7);
  } else if ((wave >> 1) == (W_P >> 1)) {
    const int pw = wave - W_P;
    LAS unsigned* Y = L + L_Y + pw * 512;
    *(LAS v4u*)(Y + lane * 8) = v4u{0u, 0u, 0u, 0u};
    *(LAS v4u*)(Y + lane * 8 + 4) = v4u{0u, 0u, 0u, 0u};
    const int nA = min(162, D - rowA), sh = pw == 0 ? 0 : (rowA & 2);
    const int ya = YZ + sh + (pw == 0 ? (s >> 2) * 144 : rowA), yb = pw == 0 ? ya : (nA == 162 ? 0 : YZ + sh - nA);
    const LAS char* ap = (const LAS char*)Y + ((c < 8 ? ya : yb) + g4 * 4) * 2;
    const LAS char* bp = (const LAS char*)(L + (pw == 0 ? L_WQ : L_W1)) + (g4 * 4 + (c >> 2)) * 128 + (c & 3) * 8;
    const int key = (g4 & 1) * 2 + (c >> 3);
    const int gA = pw == 0 ? WS_Q + j * 256 : WS_H + (lcA * 5 + j - (32 * lcA) / 9) * 256;
    int gout = g4 == 0 ? gA + c * 16 : (g4 == 2 && pw == 1 && nA < 162 ? WS_H + (lcA + 1) * 5 * 256 + c * 16 : OOB);
    PIN(gout);
    VM_DRAIN();
    STAMP(1);
    unsigned xp[5];
#pragma unroll
    for (int i = 0; i < 5; ++i) xp[i] = L[L_XR + pw * 1024 + i * 64 + lane];
    const unsigned tag = (L[L_XR + pw * 1024 + 512] + 1u) & TAG8;
#pragma nounroll
    for (int rep = 0; rep <= pw; ++rep) layernorm(xp, lane);
#pragma unroll
    for (int i = 0; i < 5; ++i)
      if (i < 4 || lane < 32) Y[(YZ + sh) / 2 + i * 64 + lane] = xp[i];
    v4u af[6];
#pragma unroll
    for (int ks = 0; ks < 6; ++ks) {
      const v2u lo = *(const LAS v2u*)(ap + ks * 64), hi = *(const LAS v2u*)(ap + ks * 64 + 32);
      af[ks] = v4u{lo.x, lo.y, hi.x, hi.y};
    }
    if (pw == 0) { af[4].z = 0u; af[4].w = 0u; }
    af[5] = v4u{g4 == 0 ? af[5].x : 0u, 0u, 0u, 0u};
#pragma unroll
    for (int ks = 0; ks < 6; ++ks) PIN(af[ks]);
    STAMP(2);
    lds_wait4<1>(FL + FL_PROG, pw + 1u);
    v4f acc[4];
    {
      v4u bf[5][4];
#pragma unroll
      for (int ks = 0; ks < 5; ++ks)
#pragma unroll
        for (int cg = 0; cg < 4; ++cg) {
          const LAS char* q = bp + ((cg ^ key) * 32) + ks * 4096;
          const v2u lo = __builtin_bit_cast(v2u, __builtin_amdgcn_ds_read_tr16_b64_v4i16((LAS v4s*)q));
          const v2u hi = __builtin_bit_cast(v2u, __builtin_amdgcn_ds_read_tr16_b64_v4i16((LAS v4s*)(q + 2048)));
          bf[ks][cg] = v4u{lo.x, lo.y, hi.x, hi.y};
        }
      SB();
#pragma unroll
      for (int cg = 0; cg < 4; ++cg) acc[cg] = v4f{0.f, 0.f, 0.f, 0.f};
#pragma unroll
      for (int ks = 0; ks < 5; ++ks)
#pragma unroll
        for (int cg = 0; cg < 4; ++cg) acc[cg] = mfma(af[ks], bf[ks][cg], acc[cg]);
    }
    if (pw == 1) {
#pragma unroll
      for (int cg = 0; cg < 4; ++cg) {
        const v2u lo = __builtin_bit_cast(v2u, __builtin_amdgcn_ds_read_tr16_b64_v4i16((LAS v4s*)(bp + ((cg ^ key) * 32) + 5 * 4096)));
        acc[cg] = mfma(af[5], v4u{lo.x, lo.y, 0u, 0u}, acc[cg]);
      }
    }
    for (int i = 0; i < STAG * s; ++i) __builtin_amdgcn_s_sleep(1);
    st128(seal8(v4f{acc[0][0], acc[1][0], acc[2][0], acc[3][0]}, tag), rwp, gout, 0);
    STAMP(3);
    const unsigned tpub = now();
    if (pw == 0) {
      pace<SETTLE>(tpub);
      lds_wait<2>(FL + FL_SLOT, NQ);
      v4f q;
#pragma nounroll
      for (int sp = 0; sp < SPIN_G; ++sp) {
        CBAR(); COUNT();
        v4u g[4];
#pragma unroll
        for (int i = 0; i < 4; ++i) g[i] = __builtin_amdgcn_raw_buffer_load_b128(rwq, lane * 16 + i * 1024, WS_Q + h * 4096, AUX_SC1);
        unsigned bad = 0;
        q = (unseal(g[0], tag, bad) + unseal(g[1], tag, bad)) + (unseal(g[2], tag, bad) + unseal(g[3], tag, bad));
        if (__all((bad & TAG8) == 0u)) break;
      }
      STAMP(4);
      LAS unsigned short* qd = (LAS unsigned short*)(L + L_QS) + g4 * 64 + c;
      const unsigned q01 = cvtpk(q[0], q[1]), q23 = cvtpk(q[2], q[3]);
      qd[0] = (unsigned short)q01; qd[16] = (unsigned short)(q01 >> 16); qd[32] = (unsigned short)q23; qd[48] = (unsigned short)(q23 >> 16);
      LDS_DRAIN();
      FL[FL_Q] = 1u;
    } else {
      const unsigned short bias = ((LAS unsigned short*)(L + L_XR + 1024 + 768))[((j * 18) & 7) + min(lane, 17)];
      const int f = j * 18 + lane, lc = f >> 6;
      const int cnt = lane < 18 ? (576 * lc + 575) / 162 - (32 * lc) / 9 + 1 : 0;
      const int ha = WS_H + lc * 5 * 256 + (f & 15) * 16;
      pace<SETTLE>(tpub);
      lds_wait<2>(FL + FL_SLOT, NH);
      v4f hv;
#pragma nounroll
      for (int sp = 0; sp < SPIN_G; ++sp) {
        CBAR(); COUNT();
        v4u g[5];
#pragma unroll
        for (int k = 0; k < 5; ++k) g[k] = __builtin_amdgcn_raw_buffer_load_b128(rwh, k < cnt ? ha + k * 256 : OOB, 0, AUX_SC1);
        unsigned bad = 0;
        hv = v4f{0.f, 0.f, 0.f, 0.f};
#pragma unroll
        for (int k = 0; k < 5; ++k) hv += unseal(g[k], k < cnt ? tag : 0u, bad);
        if (__all((bad & TAG8) == 0u)) break;
      }
      STAMP(4);
      const float hsum = (f & 32) ? ((f & 16) ? hv[3] : hv[2]) : ((f & 16) ? hv[1] : hv[0]);
      const float t = rbf(rbf(hsum) + asf((unsigned)bias << 16));
      const float z2 = 2.0f * 0.7978845608028654f * (t + 0.044715f * t * t * t);
      const float ge = t / (1.0f + __builtin_amdgcn_exp2f(-z2 * LOG2E));
      if (lane < 32) L[L_HS + lane] = lane < 18 ? cvtpk(ge, ge) : 0u;
      LDS_DRAIN();
      FL[FL_H] = 1u;
    }
    STAMP(5);
    VM_CLEAR();
  } else if (wave == W_C) {
    const unsigned tag32 = __builtin_amdgcn_raw_buffer_load_b32(rws, 0, 0, AUX_SC1) + 1u, tag = tag32 & TAG_MASK;
    lds_wait<1>(FL + FL_T0 + 1, 1u);
    unsigned t0 = __builtin_amdgcn_readfirstlane(FL[FL_T0]);
    const unsigned town = t0;
    auto mean_offset = [&](int off) {
      const v2u tt = __builtin_amdgcn_raw_buffer_load_b64(rws, WS_TT + off + c * 256, 0, AUX_SC1);
      const bool ok = tt.y == tag32;
      const float dsum = row_sum(ok ? (float)(int)(tt.x - town) : 0.f), dcnt = row_sum(ok ? 1.f : 0.f);
      float dv = ok ? (float)(int)(tt.x - town) : -1000.f;
      dv = fminf(dv, dpp<XOR1>(dv));
      const float dm = fmaxf(dsum * __builtin_amdgcn_rcpf(fmaxf(dcnt, 1.f)), row_max(dv) - (float)CAP);
      return __builtin_amdgcn_readfirstlane((int)fminf(fmaxf(dm, -150.f), 150.f));
    };
    if (BEACON) {
      if (lane == 0) poke(ws, WS_TB + (b >> 3) * 256 + (b & 7) * 8, town, tag32);
      v2u tb = {0u, 0u};
      if (lane < 8) tb = peek(ws, WS_TB + (b >> 3) * 256 + lane * 8);
      const float dmin = -row_max(tb.y == tag32 && lane < 8 ? (float)(int)(town - tb.x) : 0.f);
      t0 = town + min(max(__builtin_amdgcn_readfirstlane((int)dmin) + BIAS, -150), 0);
      *(volatile LAS v2u*)(FL + FL_T1) = v2u{t0, 1u};
    }
    if (s == 0) {
      lds_wait<1>(FL + FL_TS + 1, 1u);
      if (lane < 8) __builtin_amdgcn_raw_buffer_store_b64(v2u{FL[FL_TS], tag32}, rws, WS_TT + 128 + lane * 8, h * 256, AUX_SC1);
    }
    pace<TS>(town + (int)(t0 - town) / 2);
    t0 = town + mean_offset(128) - LEAD;
    *(volatile LAS v2u*)(FL + FL_T1) = v2u{t0, 2u};
    STAMP(1);
    lds_wait<4>(FL + FL_FW, 1u);
    const int hh = lane >> 5;
    v4f fw = *(LAS v4f*)(L + L_FW + hh * 4);
    int oaddr = WS_O + j * O_STRIDE + (hh * 128 + c * 8 + (g4 & 1) * 4) * 4;
    const v2u mlp = *(LAS v2u*)(L + L_G + (lane & 1) * 2);
    v4u mls = seal(v4f{asf(mlp.x), asf(mlp.y), asf(mlp.x), asf(mlp.y)}, tag);
    int maddr = lane < 4 ? WS_O + j * O_STRIDE + 1024 + lane * 16 : OOB;
    PIN(fw); PIN(oaddr); PIN(mls); PIN(maddr);
    pace<slot_time(44)>(t0);
    STAMP(2);
    v4f o;
#pragma nounroll
    for (int sp = 0; sp < SPIN_L; ++sp) {
      CBAR();
      v4u d[4];
#pragma unroll
      for (int w = 0; w < 4; ++w) d[w] = *(LAS v4u*)(L + L_ORED + w * 256 + lane * 4);
      o = __builtin_bit_cast(v4f, d[0]) * fw[0];
#pragma unroll
      for (int w = 1; w < 4; ++w) o += __builtin_bit_cast(v4f, d[w]) * fw[w];
      const float chk = (o[0] + o[1]) + (o[2] + o[3]);
      if (__all(chk == chk)) break;
      if (__all(full(d[0]) & full(d[1]) & full(d[2]) & full(d[3]))) break;
    }
    STAMP(3);
    st128(seal(o, tag), rws, oaddr, 0);
    st128(mls, rws, maddr, 0);
    STAMP(4);
    pace<TO>(now());
    FL[FL_OPUB] = 1u;
    VM_CLEAR();
  } else {
    const int aw = wave - W_A;
    *(LAS v4u*)(L + L_ORED + aw * 256 + lane * 4) = v4u{EMPTY, EMPTY, EMPTY, EMPTY};
    if (aw != 0) {
      __builtin_amdgcn_s_sleep(TOUCH);
      if (aw == 1) { touch2(vc, kvo, kvo); touch2(out, 0, 0); }
      else touch2(aw == 2 ? wo : w2, j * (aw == 2 ? 18432 : 20736), j * (aw == 2 ? 18432 : 20736) + 18400);
    }
    const bool reducer = aw == 1 && (j & 31) < 9;
    const int hb = c >> 3;
    lds_wait<4>(FL + FL_PROG + aw, 2u);
    lds_wait<1>(FL + FL_PROG + aw, 3u);
    STAMP(1);
    v4u kf[16];
#pragma unroll
    for (int t = 0; t < 4; ++t)
#pragma unroll
      for (int kk = 0; kk < 4; ++kk)
        kf[t * 4 + kk] = *(LAS v4u*)(L + L_K + (aw * 16 + t * 4 + (c & 3)) * 256 + (c >> 2) * 64 + (((kk * 4 + g4) ^ c) * 4));
#pragma unroll
    for (int i = 0; i < 16; ++i) PIN(kf[i]);
    lds_wait<0>(FL + FL_Q, 1u);
    STAMP(2);
    v4u qf[4];
#pragma unroll
    for (int kk = 0; kk < 4; ++kk) qf[kk] = *(LAS v4u*)(L + L_QS + hb * 64 + (kk * 4 + g4) * 4);
    SB();
    v4f sc[4];
#pragma unroll
    for (int t = 0; t < 4; ++t) sc[t] = v4f{0.f, 0.f, 0.f, 0.f};
#pragma unroll
    for (int t = 0; t < 4; ++t)
#pragma unroll
      for (int kk = 0; kk < 4; ++kk) sc[t] = mfma(kf[t * 4 + kk], qf[kk], sc[t]);
    float sv[4];
#pragma unroll
    for (int e = 0; e < 4; ++e) {
      const float lo = (c & 1) ? sc[1][e] : sc[0][e], hi = (c & 1) ? sc[3][e] : sc[2][e];
      sv[e] = rbf(rbf((c & 2) ? hi : lo) * 0.08838834764831845f);
    }
    float m = fmaxf(fmaxf(sv[0], sv[1]), fmaxf(sv[2], sv[3]));
    m = fmaxf(m, dpp<XOR1>(m)); m = fmaxf(m, dpp<XOR2>(m));
    m = xrow_max(m);
    float p[4];
    v4u pk;
#pragma unroll
    for (int e = 0; e < 4; ++e) { p[e] = __builtin_amdgcn_exp2f((sv[e] - m) * LOG2E); pk[e] = cvtpk(p[e], p[e]); }
    *(LAS v4u*)(L + L_P + aw * 128 + (hb * 4 + g4) * 16 + (c & 3) * 4) = pk;
    float l = (p[0] + p[1]) + (p[2] + p[3]);
    l += dpp<XOR1>(l); l += dpp<XOR2>(l);
    l = xrow_sum(l);
    if ((lane & 55) == 0) *(LAS v2u*)(L + L_ML + aw * 4 + hb * 2) = v2u{asu(m), asu(l)};
    LDS_DRAIN();
    FL[FL_P + aw] = 1u;
    STAMP(3);
    const unsigned tag32 = L[L_XR + 512] + 1u, tag = tag32 & TAG_MASK;
    if (aw == 0) {
      lds_wait4<0>(FL + FL_P, 1u);
      const int hh = (lane >> 2) & 1, w = lane & 3;
      const v2u ml = *(LAS v2u*)(L + L_ML + w * 4 + hh * 2);
      float M = asf(ml.x);
      M = fmaxf(M, dpp<XOR1>(M)); M = fmaxf(M, dpp<XOR2>(M));
      const float fw = __builtin_amdgcn_exp2f((asf(ml.x) - M) * LOG2E);
      float Ls = asf(ml.y) * fw;
      Ls += dpp<XOR1>(Ls); Ls += dpp<XOR2>(Ls);
      if (lane < 8) L[L_FW + lane] = asu(fw);
      if ((lane & 59) == 0) *(LAS v2u*)(L + L_G + hh * 2) = v2u{asu(M), asu(Ls)};
      LDS_DRAIN();
      FL[FL_FW] = 1u;
      STAMP(4);
    } else if (reducer) {
      const int red = (j >> 5) * 9 + (j & 31);
      const int ocol = red * 8 + (lane & 1) * 4 + g4;
      const unsigned xb = ((LAS unsigned short*)(L + L_XR))[ocol];
      const unsigned bb = ((LAS unsigned short*)(L + L_XR + 768))[ocol & 7];
      float base = asf(xb << 16) + asf(bb << 16);
      int oaddr = (lane & 14) == 0 ? ocol * 2 : OOB, eaddr = j == 0 && lane == 0 ? 0 : OOB;
      auto rout = RS(out, D * 2);
      PIN(base); PIN(oaddr); PIN(eaddr);
      asm volatile("" : "+s"(rout));
      lds_wait<4>(FL + FL_OPUB, 1u);
      lds_wait<0>(FL + FL_FPUB, 5u);
      pace<TF>(now());
      STAMP(4);
      v4f sum;
      bool done = false;
#pragma nounroll
      for (int sp = 0; sp < SPIN_G; ++sp) {
        CBAR(); COUNT();
        if (!done) {
          v4u pv[8];
#pragma unroll
          for (int i = 0; i < 8; ++i) pv[i] = __builtin_amdgcn_raw_buffer_load_b128(rws, (lane >> 1) * 64 + (lane & 1) * 16 + (i & 1) * 2048, WS_F + (red >> 1) * F_STRIDE + (red & 1) * 32 + (i >> 1) * 4096, AUX_SC1);
          unsigned bad = 0;
          sum = unseal(pv[0], tag, bad);
#pragma unroll
          for (int i = 1; i < 8; ++i) sum += unseal(pv[i], tag, bad);
          done = (bad & TAG_MASK) == 0u;
        }
        if (__all(done)) break;
      }
      STAMP(5);
      auto s02 = __builtin_amdgcn_permlane32_swap(asu(sum[0]), asu(sum[2]), false, false);
      auto s13 = __builtin_amdgcn_permlane32_swap(asu(sum[1]), asu(sum[3]), false, false);
      const float u0 = asf(s02[0]) + asf(s02[1]), u1 = asf(s13[0]) + asf(s13[1]);
      auto s01 = __builtin_amdgcn_permlane16_swap(asu(u0), asu(u1), false, false);
      float u = asf(s01[0]) + asf(s01[1]);
      u += dpp<ROR8>(u); u += dpp<ROR4>(u); u += dpp<XOR2>(u);
      u += base;
      __builtin_amdgcn_raw_buffer_store_b16((unsigned short)cvtpk(u, u), rout, oaddr, 0, OUT_AUX);
      __builtin_amdgcn_raw_buffer_store_b32(tag32, rws, eaddr, 0, EP_AUX);
      STAMP(6);
      __builtin_amdgcn_endpgm();
    }
    VM_CLEAR();
  }

}

extern "C" __device__ __attribute__((used, noinline)) void fused_decode_end() {}

extern "C" void kernel_launch(void* const* d_in, const int* in_sizes, int n_in, void* d_out, int out_size, void* d_ws, size_t ws_size,
                              hipStream_t stream) {
  fused_decode<<<256, NWAVE * 64, 0, stream>>>(d_in[3], d_in[1], d_in[6], d_in[2], d_in[4], d_in[8], d_ws, d_in[0], d_in[7], d_in[9], d_out);
}
```

```cpp
#include <hip/hip_runtime.h>
#include <stdint.h>
#include <type_traits>

typedef unsigned v4u __attribute__((ext_vector_type(4)));
typedef unsigned v2u __attribute__((ext_vector_type(2)));
typedef float v4f __attribute__((ext_vector_type(4)));
typedef float v2f __attribute__((ext_vector_type(2)));
typedef short v4s __attribute__((ext_vector_type(4)));
typedef __bf16 bf16x8 __attribute__((ext_vector_type(8)));
typedef __bf16 bf16x2 __attribute__((ext_vector_type(2)));
#define LAS __attribute__((address_space(3)))

constexpr int D = 576, SK = 16384, WIN = 4096, F = 4608;
constexpr int KV_BYTES = 16 * SK * 128 * 2, WQ_BYTES = D * 4096 * 2, W1_BYTES = D * F * 2;

constexpr int WS_TT = 4096;
constexpr int WS_Q = 8192;
constexpr int WS_H = WS_Q + 256 * 256;
constexpr int WS_O = WS_H + 72 * 5 * 256;
constexpr int O_STRIDE = 1152;
constexpr int WS_F = WS_O + 256 * O_STRIDE;
constexpr int F_STRIDE = 256 * 64 + 256;
constexpr int WS_END = WS_F + 36 * F_STRIDE;

constexpr int W = 11;
constexpr int BLIND = 4;
constexpr int AHEAD = 6;
constexpr int PACE10 = 149;
constexpr int PACE0 = 58;
constexpr int GAP = 200;
constexpr int FCOST = 85;
constexpr int LAG1 = 6, LAG2 = 5, LAG3 = 5;
constexpr int SMP = 12;
constexpr int RTT0 = 62;
constexpr int CAP = 60;
constexpr int NT1 = SMP + 13;
constexpr int DRIFT0 = NT1 + 6;
constexpr int NQ = 25, NH = 29;
constexpr int MLAT = 42, TOA = 120, TFA = 270;
constexpr int slot_cost(int n) { return n < 10 ? 90 : n == 10 ? 30 : n == 42 ? GAP : n == 47 ? 60 : n >= 43 ? FCOST : 100; }
constexpr int slot_time(int n) {
  if (n < AHEAD) return (n - BLIND + 1) * 10;
  int c = 0;
  for (int i = AHEAD; i < n; ++i) c += slot_cost(i);
  return PACE0 + c * PACE10 / 1000;
}
constexpr int pv_done(int n) { return n < 42 ? 0 : n == 42 ? 2 : n == 43 ? 9 : 16; }

constexpr int AUX_NT = 2, AUX_SC1 = 16, OOB = 0x7FFFFF00;
constexpr int SPIN_G = 1 << 16, SPIN_L = 1 << 22;
constexpr unsigned EMPTY = 0xFFFFFFFFu, TAG10 = 0x3FFu, TAG8 = 0xFFu;
constexpr float LOG2E = 1.4426950408889634f;

constexpr int L_WQ = 0  , L_K = 5120  , L_W1 = L_K + 16384  , L_FLAG = L_W1 + 6144  ;
constexpr int FL_Q = 0, FL_H = 1, FL_SLOT = 3, FL_PROG = 4  , FL_P = 8  , FL_OPUB = 12, FL_FPUB = 13,
              FL_T0 = 14  , FL_T1 = 16  , FL_TS = 18  ;
constexpr int L_XR = L_FLAG + 32  , L_Y = L_XR + 2 * 1024  , L_QS = L_Y + 2 * 512  ,
              L_HS = L_QS + 128  , L_ML = L_HS + 32  ,
              L_OS = L_ML + 16  , L_ORED = L_OS + 16  , L_JUNK = L_ORED + 1024  , L_P = L_JUNK + 256  ,
              L_END = L_P + 512;
constexpr int YZ = 192;

#define RS(p, n) __builtin_amdgcn_make_buffer_rsrc((void*)(p), 0, (int)(n), 0x00020000)
#define SB() __builtin_amdgcn_sched_barrier(0)
#define CBAR() asm volatile("" ::: "memory")
#define LDS_DRAIN() asm volatile("s_waitcnt lgkmcnt(0)" ::: "memory")
#define VM_DRAIN() asm volatile("s_waitcnt vmcnt(0)" ::: "memory")
#define VM_CLEAR() __builtin_amdgcn_s_waitcnt(0x0F70)
#define PIN(x) asm volatile("" : "+v"(x))
#define PAD(k, next) asm volatile(".Lpad" #k ":\n\ts_cbranch_execz .Lpad" #next)

#define NO_PAGE_BREAK(bytes) asm volatile("s_branch 1f\n\t.p2align 12,," #bytes "\n1:")

__device__ __forceinline__ float asf(unsigned u) { return __builtin_bit_cast(float, u); }
__device__ __forceinline__ unsigned asu(float f) { return __builtin_bit_cast(unsigned, f); }
__device__ __forceinline__ unsigned cvtpk(float lo, float hi) {
  return __builtin_bit_cast(unsigned, __builtin_convertvector(v2f{lo, hi}, bf16x2));
}
__device__ __forceinline__ v2f unpk(unsigned p) { return v2f{asf(p << 16), asf(p & 0xFFFF0000u)}; }
__device__ __forceinline__ float rbf(float f) { return asf(cvtpk(0.f, f)); }
template <int CTRL> __device__ __forceinline__ float dpp(float x) {
  return asf(__builtin_amdgcn_mov_dpp(asu(x), CTRL, 0xf, 0xf, true));
}
constexpr int XOR1 = 0xB1, XOR2 = 0x4E, XOR7 = 0x141, ROR8 = 0x128, ROR4 = 0x124;
__device__ __forceinline__ float xrow_sum(float x) {
  auto s = __builtin_amdgcn_permlane16_swap(asu(x), asu(x), false, false);
  x = asf(s[0]) + asf(s[1]);
  auto t = __builtin_amdgcn_permlane32_swap(asu(x), asu(x), false, false);
  return asf(t[0]) + asf(t[1]);
}
__device__ __forceinline__ float xrow_max(float x) {
  auto s = __builtin_amdgcn_permlane16_swap(asu(x), asu(x), false, false);
  x = fmaxf(asf(s[0]), asf(s[1]));
  auto t = __builtin_amdgcn_permlane32_swap(asu(x), asu(x), false, false);
  return fmaxf(asf(t[0]), asf(t[1]));
}
__device__ __forceinline__ float row_sum(float x) {
  x += dpp<XOR1>(x); x += dpp<XOR2>(x); x += dpp<XOR7>(x); x += dpp<ROR8>(x);
  return x;
}
__device__ __forceinline__ float row_max(float x) {
  asm("s_nop 1\n\tv_max_f32_dpp %0, %0, %0 quad_perm:[1,0,3,2] row_mask:0xf bank_mask:0xf\n\ts_nop 1\n\t"
      "v_max_f32_dpp %0, %0, %0 quad_perm:[2,3,0,1] row_mask:0xf bank_mask:0xf\n\ts_nop 1\n\t"
      "v_max_f32_dpp %0, %0, %0 row_half_mirror row_mask:0xf bank_mask:0xf\n\ts_nop 1\n\t"
      "v_max_f32_dpp %0, %0, %0 row_ror:8 row_mask:0xf bank_mask:0xf\n\ts_nop 1" : "+v"(x));
  return x;
}
__device__ __forceinline__ v4f mfma(v4u a, v4u b, v4f c) {
  return __builtin_amdgcn_mfma_f32_16x16x32_bf16(__builtin_bit_cast(bf16x8, a), __builtin_bit_cast(bf16x8, b), c, 0, 0, 0);
}
__device__ __forceinline__ v4u keep_mask(int m) {
  v4u k;
#pragma unroll
  for (int v = 0; v < 4; ++v) k[v] = ((m >> 1) == v) ? ((m & 1) ? 0xFFFF0000u : 0x0000FFFFu) : 0u;
  return k;
}

__device__ __forceinline__ v4u seal10(v4f v, unsigned tag) {
  v4u r;
#pragma unroll
  for (int i = 0; i < 4; ++i) r[i] = (asu(v[i]) & ~TAG10) | tag;
  return r;
}
__device__ __forceinline__ v4u seal8(v4f v, unsigned tag) {
  v4u r;
#pragma unroll
  for (int i = 0; i < 4; ++i) { const unsigned w = asu(v[i]); r[i] = (((w & 0x7F800000u) == 0x7F800000u ? w : w + 0x80u) & ~TAG8) | tag; }
  return r;
}
__device__ __forceinline__ v4f unseal(v4u w, unsigned tag, unsigned& bad) {
  const v4u x = w ^ tag;
  bad |= (x.x | x.y) | (x.z | x.w);
  return __builtin_bit_cast(v4f, x);
}
__device__ __forceinline__ v4u ld128(__amdgpu_buffer_rsrc_t r, int voff, int soff) {
  return __builtin_amdgcn_raw_buffer_load_b128(r, voff, soff, AUX_SC1);
}
__device__ __forceinline__ void st128(v4u d, __amdgpu_buffer_rsrc_t r, int voff) {
  __builtin_amdgcn_raw_buffer_store_b128(d, r, voff, 0, AUX_SC1);
  asm volatile("s_nop 1" ::"v"(d) : "memory");
}

template <int NAP> __device__ __forceinline__ void lds_wait(volatile LAS unsigned* f, unsigned need) {
#pragma nounroll
  for (int sp = 0; sp < SPIN_L; ++sp) {
    if ((unsigned)__builtin_amdgcn_readfirstlane(*f) >= need) break;
    if (NAP) __builtin_amdgcn_s_sleep(NAP);
  }
  CBAR();
}
template <int NAP> __device__ __forceinline__ void lds_wait4(volatile LAS unsigned* f, unsigned need) {
#pragma nounroll
  for (int sp = 0; sp < SPIN_L; ++sp) {
    const v4u v = *(volatile LAS v4u*)f;
    if ((unsigned)__builtin_amdgcn_readfirstlane(min(min(v.x, v.y), min(v.z, v.w))) >= need) break;
    if (NAP) __builtin_amdgcn_s_sleep(NAP);
  }
  CBAR();
}
template <int N> __device__ __forceinline__ void vmwait() { asm volatile("s_waitcnt vmcnt(%0)" ::"i"(N) : "memory"); }
__device__ __forceinline__ unsigned now() {
  unsigned long long t;
  asm volatile("s_memrealtime %0\n\ts_waitcnt lgkmcnt(0)" : "=s"(t)::"memory");
  return (unsigned)t;
}
template <int T> __device__ __forceinline__ void pace(unsigned t0) {
  asm volatile("1:\n\ts_memrealtime s[90:91]\n\ts_waitcnt lgkmcnt(0)\n\ts_sub_u32 s90, s90, %0\n\ts_cmpk_lt_u32 s90, %1\n\ts_cbranch_scc1 1b"
               ::"s"(__builtin_amdgcn_readfirstlane(t0)), "i"(T) : "memory", "scc", "s90", "s91");
}

template <int T> __device__ __forceinline__ void pace_nap(unsigned t0) {
  asm volatile("1:\n\ts_sleep 3\n\ts_memrealtime s[90:91]\n\ts_waitcnt lgkmcnt(0)\n\ts_sub_u32 s90, s90, %0\n\ts_cmpk_lt_u32 s90, %1\n\ts_cbranch_scc1 1b"
               ::"s"(__builtin_amdgcn_readfirstlane(t0)), "i"(T) : "memory", "scc", "s90", "s91");
}

__device__ __forceinline__ void dma(__amdgpu_buffer_rsrc_t r, int voff, int soff, int ldsaddr) {
  asm volatile("s_mov_b32 m0, %0\n\ts_nop 0\n\tbuffer_load_dwordx4 %1, %2, %3 offen nt lds" ::"s"(ldsaddr), "v"(voff), "s"(r), "s"(soff) : "memory");
}
__device__ __forceinline__ void dma_dev(__amdgpu_buffer_rsrc_t r, int voff, int ldsaddr) {
  asm volatile("s_mov_b32 m0, %0\n\ts_nop 0\n\tbuffer_load_dwordx4 %1, %2, 0 offen sc1 lds" ::"s"(ldsaddr), "v"(voff), "s"(r) : "memory");
}
__device__ __forceinline__ void touch_and_barrier(const void* p, unsigned o0, unsigned o1, unsigned o2, const void* q, unsigned o3) {
  unsigned d0, d1, d2, d3;
  asm volatile("s_load_dword %0, %4, %5\n\ts_load_dword %1, %4, %6\n\ts_load_dword %2, %4, %7\n\ts_load_dword %3, %8, %9\n\t"
               "s_barrier\n\ts_waitcnt lgkmcnt(0)"
               : "=&s"(d0), "=&s"(d1), "=&s"(d2), "=&s"(d3) : "s"(p), "s"(o0), "s"(o1), "s"(o2), "s"(q), "s"(o3) : "memory");
}
template <int I, int N, class Fn> __device__ __forceinline__ void static_for(Fn&& fn) {
  if constexpr (I < N) { fn(std::integral_constant<int, I>{}); static_for<I + 1, N>(fn); }
}

__device__ __forceinline__ void layernorm(unsigned (&p)[5], int lane) {
  const float c0 = asf(__builtin_amdgcn_readfirstlane(p[0]) << 16);
  v2f v[5], d[5];
#pragma unroll
  for (int i = 0; i < 5; ++i) { v[i] = unpk(p[i]); d[i] = v[i] - c0; }
  if (lane >= 32) d[4] = v2f{0.f, 0.f};
  const v2f s1v = (d[0] + d[1]) + (d[2] + d[3]) + d[4];
  v2f s2v = d[0] * d[0];
#pragma unroll
  for (int i = 1; i < 5; ++i) s2v = __builtin_elementwise_fma(d[i], d[i], s2v);
  const float s1 = xrow_sum(row_sum(s1v.x + s1v.y)) * (1.0f / D), s2 = xrow_sum(row_sum(s2v.x + s2v.y)) * (1.0f / D);
  const float var = fmaxf(fmaf(-s1, s1, s2), 0.f);
  const float mb = rbf(c0 + s1), sd = rbf(__builtin_amdgcn_sqrtf(rbf(rbf(var) + 1e-5f)));
  const float inv = __builtin_amdgcn_rcpf(sd);
#pragma unroll
  for (int i = 0; i < 5; ++i) {
    const v2f t = v[i] - mb;
    const v2f u = v2f{rbf(t.x), rbf(t.y)} * inv;
    p[i] = cvtpk(u.x, u.y);
  }
}
__device__ __forceinline__ float split_weight(v4f ml) {
  const float g = __builtin_amdgcn_exp2f((ml[0] - row_max(ml[0])) * LOG2E);
  return g * __builtin_amdgcn_rcpf(row_sum(ml[1] * g));
}

constexpr int W_F4 = 4, W_C = 5, W_P = 6, W_A = 8, W_X = 12, NWAVE = 13;

extern "C" __global__ void __launch_bounds__(NWAVE * 64, 4)
fused_decode(const void* __restrict__ wq, const void* __restrict__ x, void* __restrict__ ws, const void* __restrict__ kc,
             const void* __restrict__ w1, const void* __restrict__ vc, const void* __restrict__ wo, const void* __restrict__ w2,
             const void* __restrict__ b1, const void* __restrict__ b2, void* __restrict__ out) {
  __shared__ __attribute__((aligned(16))) unsigned smem[L_END];
  LAS unsigned* L = (LAS unsigned*)smem;
  volatile LAS unsigned* FL = (volatile LAS unsigned*)smem + L_FLAG;
  const int tid = threadIdx.x, lane = tid & 63, wave = __builtin_amdgcn_readfirstlane(tid >> 6);
  const int b = blockIdx.x, j = (b & 7) * 32 + (b >> 3), h = j >> 4, s = j & 15;
  const int r8 = lane >> 3, c8 = lane & 7, g4 = lane >> 4, c = lane & 15;
  const int lcA = (j * 9) >> 5, rowA = 18 * ((j * 9) & 31);
  const int lds0 = (int)(unsigned)(uintptr_t)L;
  if ((wave >> 1) == (W_P >> 1)) {
    __builtin_amdgcn_s_setprio(3);
    int dst = lds0 + (L_XR + (wave & 1) * 1024) * 4, v0 = lane * 16, v1 = lane * 16 + 1024;
    asm volatile("" : "+v"(v0), "+v"(v1), "+s"(dst));
    dma_dev(RS(x, D * 2), v0, dst);
    dma_dev(RS(x, D * 2), v1, dst + 1024);
    dma_dev(RS(ws, 16), v0, dst + 2048);
  }
  const int qs = (s >> 2) * 144 * 8192 + h * 512 + (s & 3) * 128;
  const int cs = (c8 ^ (r8 & 6)) * 16;
  if (wave < 4) {
    int v0 = (wave * 8 + r8) * 8192 + cs, l0 = lds0 + L_WQ * 4 + wave * 1024, q0 = qs;
    asm volatile("" : "+v"(v0), "+s"(l0), "+s"(q0));
    const auto rq = RS(wq, WQ_BYTES);
#pragma unroll
    for (int n = 0; n < BLIND; ++n) dma(rq, v0, q0 + n * 32 * 8192, l0 + n * 4096);
  }
  const unsigned tstart = (unsigned)__builtin_amdgcn_s_memrealtime();
  if (wave == W_C) {
    unsigned long long pc;
    unsigned pcoff, len;
    asm volatile("s_getpc_b64 %0\n.Lpc%=:\n\ts_mov_b32 %1, .Lpc%=-fused_decode\n\ts_mov_b32 %2, fused_decode_end-fused_decode"
                 : "=s"(pc), "=s"(pcoff), "=s"(len));
    dma_dev(RS(pc - pcoff, len), lane < 48 ? lane * 16 + (b >> 3) * 768 : OOB, lds0 + L_JUNK * 4);
  }
  const unsigned kvo = (h * SK + (SK - WIN) + s * 16) * 256;
  if (wave == W_A) {
    const unsigned c1 = lcA * 128;
    touch_and_barrier(w1, rowA * 9216 + c1, min(rowA + 161, D - 1) * 9216 + c1, c1, kc, kvo);
  } else if (wave >= 4) {
    if (wave == W_F4 && lane < 32) L[L_FLAG + lane] = 0u;
    LDS_DRAIN();
    __builtin_amdgcn_s_barrier();
    CBAR();
  }
  const auto rws = RS(ws, WS_END);

  if (__builtin_expect(wave <= W_F4, 1)) {
    v4u wf[9], ha[5], mskf;
    int vo, v2, faddr;
    unsigned tag;
    auto prep_addr = [&]() {
      const bool live = wave < 4 || c < 8;
      vo = live ? g4 * 4608 + wave * 256 + c * 16 : OOB;
      v2 = live ? g4 * 1152 + wave * 256 + c * 16 : OOB - 5 * 4608;
      faddr = lane < 32 && live ? WS_F + (wave * 8 + (c >> 1)) * F_STRIDE + j * 64 + (c & 1) * 32 + g4 * 16 : OOB;
      mskf = keep_mask(c);
      PIN(vo); PIN(v2); PIN(faddr); PIN(mskf);
    };
    auto prep_h = [&]() {
      lds_wait<0>(FL + FL_H, 1u);
      unsigned hv[5];
      const unsigned ep = L[L_XR + 1024 + 512];
#pragma unroll
      for (int rg = 0; rg < 5; ++rg) hv[rg] = L[L_HS + rg * 4 + g4];
      SB();
      tag = (ep + 1u) & TAG10;
#pragma unroll
      for (int rg = 0; rg < 5; ++rg) ha[rg] = mskf & hv[rg];
      asm volatile("" : "+v"(tag), "+v"(ha[0]), "+v"(ha[1]), "+v"(ha[2]), "+v"(ha[3]), "+v"(ha[4]));
    };
    if (__builtin_expect(wave < 4, 1)) {
      const auto rq = RS(wq, WQ_BYTES), rk = RS(kc, KV_BYTES), rv = RS(vc, KV_BYTES), r1 = RS(w1, W1_BYTES);
      const int kvb = kvo + wave * 1024;
      __amdgpu_buffer_rsrc_t ro, r2;
      v4u vv[16], pk[4], mskv, alast;
      v4f acc = {0.f, 0.f, 0.f, 0.f};
      unsigned t0 = tstart;
      int dacc = 0, dstep = 0;
      static_for<BLIND, 52>([&](auto nc) {
        constexpr int n = nc;
        if (n == BLIND) __builtin_amdgcn_s_barrier();
        if (n == AHEAD) {
          vmwait<AHEAD - 1>();
          const unsigned t1 = now() - slot_time(n);
          if ((int)(t1 - t0) > 0) t0 = t1;
          if (wave == 0) *(volatile LAS v2u*)(FL + FL_T0) = v2u{t0, 1u};
        }
        if (n >= W) vmwait<W - 1>();
        if constexpr (n >= DRIFT0 && n < 43) dacc += dstep;
        if constexpr (n >= DRIFT0) pace<slot_time(n)>(t0 + ((dacc + 128) >> 8));
        else pace<slot_time(n)>(t0);
        if constexpr (n < 5) {
          dma(rq, min((n * 4 + wave) * 8 + r8, 143) * 8192 + cs, qs, lds0 + L_WQ * 4 + (n * 4 + wave) * 1024);
        } else if constexpr (n < 11) {
          constexpr int i = n - 5;
          const unsigned v = (unsigned)((rowA + min((i * 4 + wave) * 8 + r8, 161)) * 9216 + cs);
          dma(r1, (int)min(v, v - (unsigned)(W1_BYTES - 128)), lcA * 128, lds0 + L_W1 * 4 + (i * 4 + wave) * 1024);
        } else if constexpr (n < 27) {
          constexpr int i = n - 11;
          dma(rk, g4 * 256 + ((c ^ (g4 * 4 + (i & 3))) * 16), kvb + (((i & 3) * 4 + (i >> 2) + h) & 15) * 65536,
              lds0 + L_K * 4 + (wave * 16 + i) * 1024);
        } else if constexpr (n < 43) {
          constexpr int i = n - 27;
          vv[i] = __builtin_amdgcn_raw_buffer_load_b128(rv, lane * 16, kvb + (((i & 3) * 4 + (i >> 2) + h) & 15) * 65536, AUX_NT);
        } else if constexpr (n < 48) {
          wf[n - 43] = __builtin_amdgcn_raw_buffer_load_b128(r2, v2 + (n - 43) * 4608, 0, AUX_NT);
        } else {
          wf[n - 43] = __builtin_amdgcn_raw_buffer_load_b128(ro, vo, (n - 48) * 1152, AUX_NT);
        }
        SB();
        if constexpr (n == 32) { NO_PAGE_BREAK(2600); PAD(8, 9); }
        if constexpr (n == 39) PAD(9, 10);
        if constexpr (n == 42) PAD(10, 11);
        if constexpr (n == 43) PAD(11, 12);
        if constexpr (n == 44) PAD(12, 13);
        if constexpr (n == 46) PAD(13, 14);
        if constexpr (n == 48) PAD(14, 15);
        if constexpr (n == 51) PAD(15, 16);
        if (n == 4 + LAG1) { vmwait<LAG1>(); FL[FL_PROG + wave] = 1u; }
        if (n == 10 + LAG2) { vmwait<LAG2>(); FL[FL_PROG + wave] = 2u; }
        if (n == 26 + LAG3) { vmwait<LAG3>(); FL[FL_PROG + wave] = 3u; }
        if (n == SMP + 3 && wave == 0) {
          if (s == 0) {
            vmwait<3>();
            const unsigned t1 = now() - (slot_time(SMP) + RTT0);
            *(volatile LAS v2u*)(FL + FL_TS) = v2u{(int)(t1 - t0) > 0 ? t1 : t0, 1u};
          }
        }
        if (n == 21) prep_addr();
        if (n == 22) {
          mskv = keep_mask(c8);
          PIN(mskv);
          ro = RS((const char*)wo + j * 18432, 18432);
          r2 = RS((const char*)w2 + j * 20736, 20736);
          asm volatile("" : "+s"(ro), "+s"(r2));
        }
        if ((n == NQ || n == NH) && wave == 0) FL[FL_SLOT] = n;
        if constexpr (n == DRIFT0 - 1 || n == DRIFT0 + 2 || n == 37) {
          const v2u t1 = *(volatile LAS v2u*)(FL + FL_T1);
          if (__builtin_amdgcn_readfirstlane(t1.y))
            dstep = (((int)(__builtin_amdgcn_readfirstlane(t1.x) - t0) * 256 - dacc) * (4096 / (42 - n))) >> 12;
        }
        if (n == 42) {
          lds_wait<0>(FL + FL_P + wave, 1u);
#pragma unroll
          for (int t = 0; t < 4; ++t) pk[t] = *(LAS v4u*)(L + L_P + wave * 128 + ((c >> 3) * 4 + g4) * 16 + t * 4);
          alast = mskv & pk[3][3];
          PIN(alast);
        }
        if constexpr (n >= 42 && n <= 44) {
#pragma unroll
          for (int i = pv_done(n - 1); i < pv_done(n); ++i) acc = mfma(i == 15 ? alast : mskv & pk[i >> 2][i & 3], vv[i], acc);
          if (n == 44) {
            *(volatile LAS v4f*)(L + L_ORED + wave * 256 + lane * 4) = acc;
            FL[FL_PROG + wave] = 4u;
          }
        }
        if (n == 47) prep_h();
        SB();
      });
    } else {
      prep_addr();
      const auto ro = RS((const char*)wo + j * 18432, 18432), r2 = RS((const char*)w2 + j * 20736, 20736);
      lds_wait<4>(FL + FL_T1 + 1, 1u);
      const unsigned t0 = __builtin_amdgcn_readfirstlane(FL[FL_T1]);
      pace_nap<slot_time(43) - 12>(t0);
      static_for<0, 9>([&](auto nc) {
        constexpr int n = nc;
        pace<slot_time(43 + n) - (n ? 6 : 0)>(t0);
        if constexpr (n < 5) wf[n] = __builtin_amdgcn_raw_buffer_load_b128(r2, v2 + n * 4608, 0, AUX_NT);
        else wf[n] = __builtin_amdgcn_raw_buffer_load_b128(ro, vo, (n - 5) * 1152, AUX_NT);
        SB();
        if constexpr (n == 1) PAD(18, 19);
        if constexpr (n == 5) PAD(19, 20);
        if (n == 4) prep_h();
        SB();
      });
      lds_wait<0>(FL + FL_OPUB, 1u);
      VM_CLEAR();
    }
    v4f a2 = {0.f, 0.f, 0.f, 0.f};
#pragma unroll
    for (int rg = 0; rg < 5; ++rg) a2 = mfma(ha[rg], wf[rg], a2);
    PIN(a2);
    SB();
    PAD(16, 17);
    v4u od;
#pragma nounroll
    for (int sp = 0; sp < SPIN_L; ++sp) {
      od = *(volatile LAS v4u*)(L + L_OS + g4 * 4);
      if (__all(max(max(od.x, od.y), max(od.z, od.w)) != EMPTY)) break;
    }
#pragma unroll
    for (int rg = 0; rg < 4; ++rg) a2 = mfma(mskf & od[rg], wf[5 + rg], a2);
    PAD(17, 18);
    st128(seal10(a2, tag), rws, faddr);
    if (lane == 0) __hip_atomic_fetch_add(L + L_FLAG + FL_FPUB, 1u, __ATOMIC_RELAXED, __HIP_MEMORY_SCOPE_WORKGROUP);
  } else if ((wave >> 1) == (W_P >> 1)) {
    NO_PAGE_BREAK(2300);
    const int pw = wave - W_P;
    LAS unsigned* Y = L + L_Y + pw * 512;
    *(LAS v4u*)(Y + lane * 8) = v4u{0u, 0u, 0u, 0u};
    *(LAS v4u*)(Y + lane * 8 + 4) = v4u{0u, 0u, 0u, 0u};
    const int nA = min(162, D - rowA), sh = pw == 0 ? 0 : (rowA & 2);
    const int ya = YZ + sh + (pw == 0 ? (s >> 2) * 144 : rowA), yb = pw == 0 ? ya : (nA == 162 ? 0 : YZ + sh - nA);
    const LAS char* ap = (const LAS char*)Y + ((c < 8 ? ya : yb) + g4 * 4) * 2;
    const LAS char* bp = (const LAS char*)(L + (pw == 0 ? L_WQ : L_W1)) + (g4 * 4 + (c >> 2)) * 128 + (c & 3) * 8;
    const int key = (g4 & 1) * 2 + (c >> 3);
    const int gA = pw == 0 ? WS_Q + j * 256 : WS_H + (lcA * 5 + j - (32 * lcA) / 9) * 256;
    int gout = g4 == 0 ? gA + c * 16 : (g4 == 2 && pw == 1 && nA < 162 ? WS_H + (lcA + 1) * 5 * 256 + c * 16 : OOB);
    PIN(gout);
    VM_DRAIN();
    unsigned xp[5];
#pragma unroll
    for (int i = 0; i < 5; ++i) xp[i] = L[L_XR + pw * 1024 + i * 64 + lane];
    const unsigned tag = (L[L_XR + pw * 1024 + 512] + 1u) & TAG8;
#pragma nounroll
    for (int rep = 0; rep <= pw; ++rep) layernorm(xp, lane);
#pragma unroll
    for (int i = 0; i < 5; ++i)
      if (i < 4 || lane < 32) Y[(YZ + sh) / 2 + i * 64 + lane] = xp[i];
    CBAR();
    v4u af[6];
#pragma unroll
    for (int ks = 0; ks < 6; ++ks) {
      const v2u lo = *(const LAS v2u*)(ap + ks * 64), hi = *(const LAS v2u*)(ap + ks * 64 + 32);
      af[ks] = v4u{lo.x, lo.y, hi.x, hi.y};
    }
    if (pw == 0) { af[4].z = 0u; af[4].w = 0u; }
    af[5] = v4u{g4 == 0 ? af[5].x : 0u, 0u, 0u, 0u};
#pragma unroll
    for (int ks = 0; ks < 6; ++ks) PIN(af[ks]);
    lds_wait4<1>(FL + FL_PROG, pw + 1u);
    v4f acc[4];
    {
      v4u bf[5][4];
#pragma unroll
      for (int ks = 0; ks < 5; ++ks)
#pragma unroll
        for (int cg = 0; cg < 4; ++cg) {
          const LAS char* q = bp + ((cg ^ key) * 32) + ks * 4096;
          const v2u lo = __builtin_bit_cast(v2u, __builtin_amdgcn_ds_read_tr16_b64_v4i16((LAS v4s*)q));
          const v2u hi = __builtin_bit_cast(v2u, __builtin_amdgcn_ds_read_tr16_b64_v4i16((LAS v4s*)(q + 2048)));
          bf[ks][cg] = v4u{lo.x, lo.y, hi.x, hi.y};
        }
      SB();
#pragma unroll
      for (int cg = 0; cg < 4; ++cg) acc[cg] = v4f{0.f, 0.f, 0.f, 0.f};
#pragma unroll
      for (int ks = 0; ks < 5; ++ks)
#pragma unroll
        for (int cg = 0; cg < 4; ++cg) acc[cg] = mfma(af[ks], bf[ks][cg], acc[cg]);
    }
    if (pw == 1) {
#pragma unroll
      for (int cg = 0; cg < 4; ++cg) {
        const v2u lo = __builtin_bit_cast(v2u, __builtin_amdgcn_ds_read_tr16_b64_v4i16((LAS v4s*)(bp + ((cg ^ key) * 32) + 5 * 4096)));
        acc[cg] = mfma(af[5], v4u{lo.x, lo.y, 0u, 0u}, acc[cg]);
      }
    }
    st128(seal8(v4f{acc[0][0], acc[1][0], acc[2][0], acc[3][0]}, tag), rws, gout);
    dma_dev(RS(pw ? (const char*)b1 : (const char*)b2, pw ? F * 2 : D * 2),
            lane < 4 ? lane * 16 + (pw ? (j * 36) & ~15 : ((j >> 5) * 9 + min(j & 31, 8)) * 16) : OOB, lds0 + (L_XR + pw * 1024 + 768) * 4);
    VM_DRAIN();
    if (pw == 0) {
      lds_wait<2>(FL + FL_SLOT, NQ);
      PAD(0, 1);
      v4f q;
#pragma nounroll
      for (int sp = 0; sp < SPIN_G; ++sp) {
        CBAR();
        v4u g[4];
#pragma unroll
        for (int i = 0; i < 4; ++i) g[i] = ld128(rws, lane * 16 + i * 1024, WS_Q + h * 4096);
        unsigned bad = 0;
        q = (unseal(g[0], tag, bad) + unseal(g[1], tag, bad)) + (unseal(g[2], tag, bad) + unseal(g[3], tag, bad));
        if (__all((bad & TAG8) == 0u)) break;
      }
      PAD(1, 2);
      LAS unsigned short* qd = (LAS unsigned short*)(L + L_QS) + g4 * 64 + c;
      const unsigned q01 = cvtpk(q[0], q[1]), q23 = cvtpk(q[2], q[3]);
      qd[0] = (unsigned short)q01; qd[16] = (unsigned short)(q01 >> 16); qd[32] = (unsigned short)q23; qd[48] = (unsigned short)(q23 >> 16);
      LDS_DRAIN();
      FL[FL_Q] = 1u;
    } else {
      const unsigned short bias = ((LAS unsigned short*)(L + L_XR + 1024 + 768))[((j * 18) & 7) + min(lane, 17)];
      const int f = j * 18 + lane, lc = f >> 6;
      const int cnt = lane < 18 ? (576 * lc + 575) / 162 - (32 * lc) / 9 + 1 : 0;
      const int ha = WS_H + lc * 5 * 256 + (f & 15) * 16;
      lds_wait<2>(FL + FL_SLOT, NH);
      PAD(6, 7);
      v4f hv;
#pragma nounroll
      for (int sp = 0; sp < SPIN_G; ++sp) {
        CBAR();
        v4u g[5];
#pragma unroll
        for (int k = 0; k < 5; ++k) g[k] = ld128(rws, k < cnt ? ha + k * 256 : OOB, 0);
        unsigned bad = 0;
        hv = v4f{0.f, 0.f, 0.f, 0.f};
#pragma unroll
        for (int k = 0; k < 5; ++k) hv += unseal(g[k], k < cnt ? tag : 0u, bad);
        if (__all((bad & TAG8) == 0u)) break;
      }
      const float hsum = (f & 32) ? ((f & 16) ? hv[3] : hv[2]) : ((f & 16) ? hv[1] : hv[0]);
      PAD(7, 8);
      const float t = rbf(rbf(hsum) + asf((unsigned)bias << 16));
      const float z2 = 2.0f * 0.7978845608028654f * (t + 0.044715f * t * t * t);
      const float ge = t / (1.0f + __builtin_amdgcn_exp2f(-z2 * LOG2E));
      if (lane < 32) L[L_HS + lane] = lane < 18 ? cvtpk(ge, ge) : 0u;
      LDS_DRAIN();
      FL[FL_H] = 1u;
    }
    VM_CLEAR();
  } else if (wave == W_C) {
    __builtin_amdgcn_s_setprio(3);
    const unsigned tag32 = __builtin_amdgcn_raw_buffer_load_b32(rws, 0, 0, AUX_SC1) + 1u, tag = tag32 & TAG10;
    lds_wait<1>(FL + FL_T0 + 1, 1u);
    unsigned t0 = __builtin_amdgcn_readfirstlane(FL[FL_T0]);
    if (s == 0) {
      lds_wait<1>(FL + FL_TS + 1, 1u);
      if (lane < 16) __builtin_amdgcn_raw_buffer_store_b64(v2u{FL[FL_TS], tag32}, rws, WS_TT + lane * 8, h * 256, AUX_SC1);
    }
    pace_nap<slot_time(NT1)>(t0);
    {
      const v2u tt = __builtin_amdgcn_raw_buffer_load_b64(rws, WS_TT + c * 256, 0, AUX_SC1);
      const bool ok = tt.y == tag32;
      const float dsum = row_sum(ok ? (float)(int)(tt.x - t0) : 0.f), dcnt = row_sum(ok ? 1.f : 0.f);
      float dv = ok ? (float)(int)(tt.x - t0) : -1000.f;
      dv = fminf(dv, dpp<XOR1>(dv));
      const float dm = fmaxf(dsum * __builtin_amdgcn_rcpf(fmaxf(dcnt, 1.f)), row_max(dv) - (float)CAP);
      t0 += __builtin_amdgcn_readfirstlane((int)fminf(fmaxf(dm, -150.f), 150.f));
    }
    *(volatile LAS v2u*)(FL + FL_T1) = v2u{t0, 1u};
    const int hh = lane >> 5;
    int oaddr = WS_O + j * O_STRIDE + (hh * 128 + c * 8 + (g4 & 1) * 4) * 4;
    int maddr = (lane & 28) == 0 ? WS_O + j * O_STRIDE + 1024 + (lane & 3) * 32 + hh * 16 : OOB;
    int oga = WS_O + (h * 16 + c) * O_STRIDE + s * 64 + g4 * 16, mga = WS_O + 1024 + (h * 16 + c) * O_STRIDE + (s >> 3) * 16;
    PIN(oaddr); PIN(maddr); PIN(oga); PIN(mga);
    NO_PAGE_BREAK(1400);
    PAD(20, 21);
    lds_wait4<1>(FL + FL_P, 1u);
    const v4f m4 = *(LAS v4f*)(L + L_ML + hh * 8), l4 = *(LAS v4f*)(L + L_ML + hh * 8 + 4);
    const float M = fmaxf(fmaxf(m4[0], m4[1]), fmaxf(m4[2], m4[3]));
    v4f fw;
#pragma unroll
    for (int w = 0; w < 4; ++w) fw[w] = __builtin_amdgcn_exp2f((m4[w] - M) * LOG2E);
    const float Ls = (l4[0] * fw[0] + l4[1] * fw[1]) + (l4[2] * fw[2] + l4[3] * fw[3]);
    v4u mls = seal10(v4f{M, Ls, M, Ls}, tag);
    PIN(fw); PIN(mls);
    pace<slot_time(42) + MLAT>(t0);
    const unsigned tml = now();
    st128(mls, rws, maddr);
    PAD(21, 22);
    v4f d[4];
#pragma nounroll
    for (int sp = 0; sp < SPIN_L; ++sp) {
      const v4u f = *(volatile LAS v4u*)(FL + FL_PROG);
#pragma unroll
      for (int w = 0; w < 4; ++w) d[w] = *(volatile LAS v4f*)(L + L_ORED + w * 256 + lane * 4);
      if ((unsigned)__builtin_amdgcn_readfirstlane(f.x & f.y & f.z & f.w) == 4u) break;
    }
    st128(seal10((d[0] * fw[0] + d[1] * fw[1]) + (d[2] * fw[2] + d[3] * fw[3]), tag), rws, oaddr);
    PAD(22, 23);
    const unsigned tpub = now();
    FL[FL_OPUB] = 1u;
    pace<30>(tml);
    const v4u mw = ld128(rws, mga, 0);
    SB();
    pace<12>(tpub);
    pace<slot_time(42) + TOA>(t0);
    const v4u ow = ld128(rws, oga, 0);
    SB();
    unsigned bad = 0;
    float g = split_weight(unseal(mw, tag, bad));
    PIN(g); PIN(bad);
    SB();
    PAD(23, 24);
    v4f u = unseal(ow, tag, bad);
    if (__builtin_expect(!__all((bad & TAG10) == 0u), 0)) {
      v4f ml;
#pragma nounroll
      for (int sp = 0; sp < SPIN_G; ++sp) {
        CBAR();
        PAD(24, 25);
        bad = 0;
        ml = unseal(ld128(rws, mga, 0), tag, bad);
        u = unseal(ld128(rws, oga, 0), tag, bad);
        if (__all((bad & TAG10) == 0u)) break;
      }
      g = split_weight(ml);
    }
    float r[4] = {u[0] * g, u[1] * g, u[2] * g, u[3] * g};
#pragma unroll
    for (int i = 0; i < 4; ++i) PIN(r[i]);
#pragma unroll
    for (int i = 0; i < 4; ++i) r[i] += dpp<XOR1>(r[i]);
#pragma unroll
    for (int i = 0; i < 4; ++i) r[i] += dpp<XOR2>(r[i]);
#pragma unroll
    for (int i = 0; i < 4; ++i) r[i] += dpp<XOR7>(r[i]);
#pragma unroll
    for (int i = 0; i < 4; ++i) r[i] += dpp<ROR8>(r[i]);
    v4u od;
#pragma unroll
    for (int i = 0; i < 4; ++i) od[i] = min(cvtpk(r[i], r[i]), EMPTY - 1u);
    PAD(25, 26);
    *(LAS v4u*)(L + L_OS + g4 * 4) = od;
    VM_CLEAR();
  } else if (wave == W_X) {
    lds_wait<4>(FL + FL_T0 + 1, 1u);
    unsigned d0, d1, d2, d3, d4, d5;
    asm volatile("s_load_dword %0, %6, %7\n\ts_load_dword %1, %8, %9\n\ts_load_dword %2, %8, %10\n\ts_load_dword %3, %11, %12\n\t"
                 "s_load_dword %4, %11, %13\n\ts_load_dword %5, %14, 0x0\n\t"
                 "s_mov_b64 exec, 0\n\ts_branch .Lpad0\n.Lpad30:\n\ts_mov_b64 exec, -1\n\ts_waitcnt lgkmcnt(0)"
                 : "=&s"(d0), "=&s"(d1), "=&s"(d2), "=&s"(d3), "=&s"(d4), "=&s"(d5)
                 : "s"(vc), "s"(kvo), "s"(w2), "s"(j * 20736), "s"(j * 20736 + 20700), "s"(wo), "s"(j * 18432), "s"(j * 18432 + 18400), "s"(out)
                 : "memory");
  } else {
    const int aw = wave - W_A;
    L[L_OS + c] = EMPTY;
    const int hb = c >> 3;
    lds_wait<4>(FL + FL_SLOT, NQ);
    NO_PAGE_BREAK(1300);
    PAD(2, 3);
    lds_wait<1>(FL + FL_Q, 1u);
    v4u qf[4];
#pragma unroll
    for (int kk = 0; kk < 4; ++kk) { qf[kk] = *(LAS v4u*)(L + L_QS + hb * 64 + (kk * 4 + g4) * 4); }
    int ka[4];
#pragma unroll
    for (int kk = 0; kk < 4; ++kk) { ka[kk] = (L_K + (aw * 16 + (c & 3)) * 256 + (c >> 2) * 64 + (((kk * 4 + g4) ^ c) * 4)) * 4; PIN(ka[kk]); PIN(qf[kk]); }
    lds_wait<0>(FL + FL_PROG + aw, 3u);
    PAD(3, 4);
    v4u kf[16];
#pragma unroll
    for (int t = 0; t < 4; ++t)
#pragma unroll
      for (int kk = 0; kk < 4; ++kk) kf[t * 4 + kk] = *(LAS v4u*)((LAS char*)L + ka[kk] + t * 4096);
    SB();
    v4f sc[4];
#pragma unroll
    for (int t = 0; t < 4; ++t) sc[t] = v4f{0.f, 0.f, 0.f, 0.f};
#pragma unroll
    for (int t = 0; t < 4; ++t)
#pragma unroll
      for (int kk = 0; kk < 4; ++kk) sc[t] = mfma(kf[t * 4 + kk], qf[kk], sc[t]);
    float sv[4];
#pragma unroll
    for (int e = 0; e < 4; ++e) {
      const float lo = (c & 1) ? sc[1][e] : sc[0][e], hi = (c & 1) ? sc[3][e] : sc[2][e];
      sv[e] = rbf(rbf((c & 2) ? hi : lo) * 0.08838834764831845f);
    }
    float m = fmaxf(fmaxf(sv[0], sv[1]), fmaxf(sv[2], sv[3]));
    m = fmaxf(m, dpp<XOR1>(m)); m = fmaxf(m, dpp<XOR2>(m));
    m = xrow_max(m);
    PAD(4, 6);
    float p[4];
    v4u pk;
#pragma unroll
    for (int e = 0; e < 4; ++e) { p[e] = __builtin_amdgcn_exp2f((sv[e] - m) * LOG2E); pk[e] = cvtpk(p[e], p[e]); }
    *(LAS v4u*)(L + L_P + aw * 128 + (hb * 4 + g4) * 16 + (c & 3) * 4) = pk;
    float l = (p[0] + p[1]) + (p[2] + p[3]);
    l += dpp<XOR1>(l); l += dpp<XOR2>(l);
    l = xrow_sum(l);
    if ((lane & 55) == 0) { L[L_ML + hb * 8 + aw] = asu(m); L[L_ML + hb * 8 + 4 + aw] = asu(l); }
    LDS_DRAIN();
    FL[FL_P + aw] = 1u;
    if (aw == 1 && (j & 31) < 9) {
      const unsigned tag32 = L[L_XR + 512] + 1u, tag = tag32 & TAG10;
      const int red = (j >> 5) * 9 + (j & 31);
      const int ocol = red * 8 + (lane & 1) * 4 + g4;
      const unsigned xb = ((LAS unsigned short*)(L + L_XR))[ocol];
      const unsigned bb = ((LAS unsigned short*)(L + L_XR + 768))[ocol & 7];
      float base = asf(xb << 16) + asf(bb << 16);
      int oaddr = (lane & 14) == 0 ? ocol * 2 : OOB, eaddr = j == 0 && lane < 32 ? lane * 4 : OOB;
      auto rout = RS(out, D * 2);
      auto rf = RS((const char*)ws + WS_F + (red >> 1) * F_STRIDE, 256 * 64);
      int fa[4];
#pragma unroll
      for (int i = 0; i < 4; ++i) { fa[i] = (lane >> 1) * 64 + (red & 1) * 32 + (lane & 1) * 16 + i * 4096; PIN(fa[i]); }
      PIN(base); PIN(oaddr); PIN(eaddr);
      asm volatile("" : "+s"(rf), "+s"(rout));
      NO_PAGE_BREAK(1300);
      PAD(26, 27);
      lds_wait<4>(FL + FL_OPUB, 1u);
      const unsigned t1 = __builtin_amdgcn_readfirstlane(FL[FL_T1]);
      lds_wait<0>(FL + FL_FPUB, 5u);
      pace<8>(now());
      pace<slot_time(42) + TFA>(t1);
      PAD(27, 28);
      auto gather = [&](unsigned& bad) {
        v4u pv[8];
#pragma unroll
        for (int i = 0; i < 8; ++i) pv[i] = ld128(rf, fa[i >> 1] + (i & 1) * 2048, 0);
        SB();
        v4f t = unseal(pv[0], tag, bad);
#pragma unroll
        for (int i = 1; i < 8; ++i) { PIN(bad); SB(); t += unseal(pv[i], tag, bad); }
        return t;
      };
      unsigned bad = 0;
      v4f sum = gather(bad);
      if (__builtin_expect(!__all((bad & TAG10) == 0u), 0)) {
#pragma nounroll
        for (int sp = 0; sp < SPIN_G; ++sp) {
          CBAR();
          PAD(28, 29);
          if ((bad & TAG10) != 0u) { bad = 0; sum = gather(bad); }
          if (__all((bad & TAG10) == 0u)) break;
        }
      }
      __builtin_amdgcn_raw_buffer_store_b32(tag32, rws, eaddr, 0, AUX_SC1);
      PAD(29, 30);
      auto s02 = __builtin_amdgcn_permlane32_swap(asu(sum[0]), asu(sum[2]), false, false);
      auto s13 = __builtin_amdgcn_permlane32_swap(asu(sum[1]), asu(sum[3]), false, false);
      const float u0 = asf(s02[0]) + asf(s02[1]), u1 = asf(s13[0]) + asf(s13[1]);
      auto s01 = __builtin_amdgcn_permlane16_swap(asu(u0), asu(u1), false, false);
      float u = asf(s01[0]) + asf(s01[1]);
      u += dpp<ROR8>(u); u += dpp<ROR4>(u); u += dpp<XOR2>(u);
      u += base;
      __builtin_amdgcn_raw_buffer_store_b16((unsigned short)cvtpk(u, u), rout, oaddr, 0, AUX_SC1);
      __builtin_amdgcn_endpgm();
    }
    VM_CLEAR();
  }
}

extern "C" __device__ __attribute__((used, noinline)) void fused_decode_end() {}

extern "C" void kernel_launch(void* const* d_in, const int* in_sizes, int n_in, void* d_out, int out_size, void* d_ws, size_t ws_size,
                              hipStream_t stream) {
  fused_decode<<<256, NWAVE * 64, 0, stream>>>(d_in[3], d_in[0], d_ws, d_in[1], d_in[6], d_in[2], d_in[4], d_in[8], d_in[7], d_in[9], d_out);
}
```
